```python
import torch

def _softmax(x, axis=-1):
    return torch.softmax(x, dim=axis)
def _gelu(x):
    return torch.nn.functional.gelu(x, approximate="tanh")
def _silu(x):
    return torch.nn.functional.silu(x)
def _sigmoid(x):
    return torch.sigmoid(x)
def _layernorm(x, axis=-1, eps=1e-5):
    m = x.mean(dim=axis, keepdim=True)
    v = x.var(dim=axis, keepdim=True, unbiased=False)
    return (x - m) / torch.sqrt(v + eps)
def _scatter_add(tgt, idx, src, axis=0):
    return tgt.clone().index_add_(axis, idx, src)
def _segment_sum(src, seg, n):
    out = torch.zeros((n,) + src.shape[1:], dtype=src.dtype)
    return out.index_add_(0, seg, src)
def _pad(x, pw):
    flat = tuple(v for pair in reversed(pw) for v in pair)
    return torch.nn.functional.pad(x, flat)

kernel_name = 'standalone__dense_activation_mish__prompt'

def setup_inputs():

    _w = (lambda a: (lambda t: t.to(torch.bfloat16) if t.is_floating_point() else t)(torch.as_tensor(a)))
    B, S, D, F = 1, 2048, 512, 1792
    return {
        "x": _w((lambda *s: torch.randn(*s, dtype=torch.bfloat16) * 0.05)(B, S, D)),
        "w1": _w((lambda *s: torch.randn(*s, dtype=torch.bfloat16) * 3.2)(D, F) * (D ** -0.5)),
        "b1": _w((lambda *s: torch.randn(*s, dtype=torch.bfloat16) * 0.05)(F)),
        "w2": _w((lambda *s: torch.randn(*s, dtype=torch.bfloat16) * 0.05)(F, D) * (F ** -0.5)),
    }

def reference(x, w1, b1, w2):
    h = torch.einsum("bsd,df->bsf", x, w1) + b1
    sp = torch.log1p(torch.exp(-torch.abs(h))) + torch.where(h > 0, h, torch.zeros_like(h))
    act = h * torch.tanh(sp)
    return torch.einsum("bsf,fd->bsd", act, w2)
```

```cpp
#include <hip/hip_runtime.h>
#include <hip/hip_bf16.h>
typedef short bf16x8 __attribute__((ext_vector_type(8)));
typedef float f32x16 __attribute__((ext_vector_type(16)));
typedef float f32x2 __attribute__((ext_vector_type(2)));
typedef __bf16 bf16x2 __attribute__((ext_vector_type(2)));
constexpr int S=2048, D=512, F=1792;

__device__ __forceinline__ float mish(float h){
  float e = __builtin_amdgcn_exp2f(fminf(h*1.44269504088896f, 40.f));
  float n = e*(e+2.f);
  return h*n*__builtin_amdgcn_rcpf(n+2.f);
}
__device__ __forceinline__ float bf2f(unsigned short u){ return __uint_as_float(((unsigned)u)<<16); }
__device__ __forceinline__ unsigned short f2bf(float f){ __bf16 b=(__bf16)f; return __builtin_bit_cast(unsigned short,b); }

template<int K,int N,bool ACT>
__global__ __launch_bounds__(256) void gemm(const unsigned short* __restrict__ A,const unsigned short* __restrict__ B,const unsigned short* __restrict__ bias,unsigned short* __restrict__ C){
  __shared__ __attribute__((aligned(16))) unsigned short As[64][40];
  __shared__ __attribute__((aligned(16))) unsigned short Bs[64][40];
  int tid=threadIdx.x, lane=tid&63, w=tid>>6;
  int bm=blockIdx.y*64, bn=blockIdx.x*64;
  int wm=(w>>1)*32, wn=(w&1)*32;
  f32x16 acc={};
  for(int k0=0;k0<K;k0+=32){
    { int r=tid>>2,c=(tid&3)*8;
      *(bf16x8*)&As[r][c]=*(const bf16x8*)&A[(size_t)(bm+r)*K+k0+c]; }
    { int r=tid>>3,c=(tid&7)*8;
      bf16x8 v=*(const bf16x8*)&B[(size_t)(k0+r)*N+bn+c];
      for(int j=0;j<8;j++) Bs[c+j][r]=v[j]; }
    __syncthreads();
    for(int kk=0;kk<32;kk+=16){
      bf16x8 a=*(bf16x8*)&As[wm+(lane&31)][kk+8*(lane>>5)];
      bf16x8 b=*(bf16x8*)&Bs[wn+(lane&31)][kk+8*(lane>>5)];
      acc=__builtin_amdgcn_mfma_f32_32x32x16_bf16(a,b,acc,0,0,0);
    }
    __syncthreads();
  }
  int col=bn+wn+(lane&31);
  float bv = ACT? bf2f(bias[col]):0.f;
  for(int r=0;r<16;r++){
    int row=bm+wm+(r&3)+8*(r>>2)+4*(lane>>5);
    float v=acc[r];
    if(ACT) v=mish(v+bv);
    C[(size_t)row*N+col]=f2bf(v);
  }
}

extern "C" void kernel_launch(void* const* d_in, const int* in_sizes, int n_in,
                              void* d_out, int out_size, void* d_ws, size_t ws_size,
                              hipStream_t stream) {
  const unsigned short* x=(const unsigned short*)d_in[0];
  const unsigned short* w1=(const unsigned short*)d_in[1];
  const unsigned short* b1=(const unsigned short*)d_in[2];
  const unsigned short* w2=(const unsigned short*)d_in[3];
  unsigned short* h=(unsigned short*)d_ws;
  gemm<D,F,true><<<dim3(F/64,S/64),256,0,stream>>>(x,w1,b1,h);
  gemm<F,D,false><<<dim3(D/64,S/64),256,0,stream>>>(h,w2,nullptr,(unsigned short*)d_out);
}
```

```cpp
#include <hip/hip_runtime.h>
#include <hip/hip_bf16.h>
typedef short bf16x8 __attribute__((ext_vector_type(8)));
typedef float f32x16 __attribute__((ext_vector_type(16)));
typedef float f32x2 __attribute__((ext_vector_type(2)));
typedef __bf16 bf16x2 __attribute__((ext_vector_type(2)));
constexpr int S=2048, D=512, F=1792;

__device__ __forceinline__ float mish(float h){
  float e = __builtin_amdgcn_exp2f(fminf(h*1.44269504088896f, 40.f));
  float n = e*(e+2.f);
  return h*n*__builtin_amdgcn_rcpf(n+2.f);
}
__device__ __forceinline__ float bf2f(unsigned short u){ return __uint_as_float(((unsigned)u)<<16); }
__device__ __forceinline__ unsigned short f2bf(float f){ __bf16 b=(__bf16)f; return __builtin_bit_cast(unsigned short,b); }

template<int K,int N,bool ACT>
__global__ __launch_bounds__(256) void gemm(const unsigned short* __restrict__ A,const unsigned short* __restrict__ B,const unsigned short* __restrict__ bias,unsigned short* __restrict__ C){
  __shared__ __attribute__((aligned(16))) unsigned short As[64][40];
  __shared__ __attribute__((aligned(16))) unsigned short Bs[64][40];
  int tid=threadIdx.x, lane=tid&63, w=tid>>6;
  int bm=blockIdx.y*64, bn=blockIdx.x*64;
  int wm=(w>>1)*32, wn=(w&1)*32;
  f32x16 acc={};
  for(int k0=0;k0<K;k0+=32){
    { int r=tid>>2,c=(tid&3)*8;
      *(bf16x8*)&As[r][c]=*(const bf16x8*)&A[(size_t)(bm+r)*K+k0+c]; }
    { int r=tid>>3,c=(tid&7)*8;
      bf16x8 v=*(const bf16x8*)&B[(size_t)(k0+r)*N+bn+c];
      for(int j=0;j<8;j++) Bs[c+j][r]=v[j]; }
    __syncthreads();
    for(int kk=0;kk<32;kk+=16){
      bf16x8 a=*(bf16x8*)&As[wm+(lane&31)][kk+8*(lane>>5)];
      bf16x8 b=*(bf16x8*)&Bs[wn+(lane&31)][kk+8*(lane>>5)];
      acc=__builtin_amdgcn_mfma_f32_32x32x16_bf16(a,b,acc,0,0,0);
    }
    __syncthreads();
  }
  int col=bn+wn+(lane&31);
  float bv = ACT? bf2f(bias[col]):0.f;
  for(int r=0;r<16;r++){
    int row=bm+wm+(r&3)+8*(r>>2)+4*(lane>>5);
    float v=acc[r];
    if(ACT) v=mish(v+bv);
    C[(size_t)row*N+col]=f2bf(v);
  }
}


__global__ __launch_bounds__(64) void probe(const char* p, unsigned* sink, int n, int stride){
  unsigned dep=0; unsigned acc=0;
  for(int i=0;i<n;i++){
    unsigned v;
    const char* a = p + (size_t)i*stride + dep;
    asm volatile("global_load_dword %0, %1, off\n\ts_waitcnt vmcnt(0)\n\tv_and_b32 %0, 0, %0":"=&v"(v):"v"(a):"memory");
    dep=v; acc+=v;
  }
  if(acc==12345) sink[0]=acc;
}

extern "C" void kernel_launch(void* const* d_in, const int* in_sizes, int n_in,
                              void* d_out, int out_size, void* d_ws, size_t ws_size,
                              hipStream_t stream) {
  const unsigned short* x=(const unsigned short*)d_in[0];
  const unsigned short* w1=(const unsigned short*)d_in[1];
  const unsigned short* b1=(const unsigned short*)d_in[2];
  const unsigned short* w2=(const unsigned short*)d_in[3];
  unsigned short* h=(unsigned short*)d_ws;
  probe<<<1,64,0,stream>>>((const char*)x,(unsigned*)d_ws+(64<<20)/4,0,7168);
  gemm<D,F,true><<<dim3(F/64,S/64),256,0,stream>>>(x,w1,b1,h);
  gemm<F,D,false><<<dim3(D/64,S/64),256,0,stream>>>(h,w2,nullptr,(unsigned short*)d_out);
}
```

```cpp
#include <hip/hip_runtime.h>
#include <hip/hip_bf16.h>
typedef short bf16x8 __attribute__((ext_vector_type(8)));
typedef float f32x4 __attribute__((ext_vector_type(4)));
constexpr int S=2048, D=512, F=1792;
__device__ __forceinline__ float mish(float h){
  float e = __builtin_amdgcn_exp2f(fminf(h*1.44269504088896f, 40.f));
  float n = e*(e+2.f);
  return h*n*__builtin_amdgcn_rcpf(n+2.f);
}
__device__ __forceinline__ float bf2f(unsigned short u){ return __uint_as_float(((unsigned)u)<<16); }
__device__ __forceinline__ unsigned short f2bf(float f){ __bf16 b=(__bf16)f; return __builtin_bit_cast(unsigned short,b); }
__global__ __launch_bounds__(512) void fused(const unsigned short* __restrict__ x,const unsigned short* __restrict__ w1,const unsigned short* __restrict__ b1,const unsigned short* __restrict__ w2,unsigned short* __restrict__ out){
  __shared__ __attribute__((aligned(16))) unsigned short hs[16][F+8];
  int tid=threadIdx.x, lane=tid&63, w=tid>>6;
  int r0=blockIdx.x*16;
  int c=lane&15,q=lane>>4;
  {
    f32x4 acc[14]={};
    for(int k0=0;k0<D;k0+=32){
      bf16x8 a=*(const bf16x8*)&x[(size_t)(r0+c)*D+k0+8*q];
      #pragma unroll
      for(int t=0;t<14;t++){
        bf16x8 b;
        #pragma unroll
        for(int j=0;j<8;j++) b[j]=w1[(size_t)(k0+8*q+j)*F+224*w+16*t+c];
        acc[t]=__builtin_amdgcn_mfma_f32_16x16x32_bf16(a,b,acc[t],0,0,0);
      }
    }
    #pragma unroll
    for(int t=0;t<14;t++){ int col=224*w+16*t+c; float bv=bf2f(b1[col]);
      for(int r=0;r<4;r++) hs[4*q+r][col]=f2bf(mish(acc[t][r]+bv)); }
  }
  __syncthreads();
  {
    f32x4 acc[4]={};
    for(int k0=0;k0<F;k0+=32){
      bf16x8 a=*(const bf16x8*)&hs[c][k0+8*q];
      #pragma unroll
      for(int t=0;t<4;t++){
        bf16x8 b;
        #pragma unroll
        for(int j=0;j<8;j++) b[j]=w2[(size_t)(k0+8*q+j)*D+64*w+16*t+c];
        acc[t]=__builtin_amdgcn_mfma_f32_16x16x32_bf16(a,b,acc[t],0,0,0);
      }
    }
    #pragma unroll
    for(int t=0;t<4;t++) for(int r=0;r<4;r++) out[(size_t)(r0+4*q+r)*D+64*w+16*t+c]=f2bf(acc[t][r]);
  }
}

extern "C" void kernel_launch(void* const* d_in, const int* in_sizes, int n_in,
                              void* d_out, int out_size, void* d_ws, size_t ws_size,
                              hipStream_t stream) {
  fused<<<128,512,0,stream>>>((const unsigned short*)d_in[0],(const unsigned short*)d_in[1],(const unsigned short*)d_in[2],(const unsigned short*)d_in[3],(unsigned short*)d_out);
}
```

```cpp
#include <hip/hip_runtime.h>
#include <hip/hip_bf16.h>
typedef short bf16x8 __attribute__((ext_vector_type(8)));
typedef float f32x4 __attribute__((ext_vector_type(4)));
constexpr int S=2048, D=512, F=1792;
__device__ __forceinline__ float mish(float h){
  float e = __builtin_amdgcn_exp2f(fminf(h*1.44269504088896f, 40.f));
  float n = e*(e+2.f);
  return h*n*__builtin_amdgcn_rcpf(n+2.f);
}
__device__ __forceinline__ float bf2f(unsigned short u){ return __uint_as_float(((unsigned)u)<<16); }
__device__ __forceinline__ unsigned short f2bf(float f){ __bf16 b=(__bf16)f; return __builtin_bit_cast(unsigned short,b); }
__global__ __launch_bounds__(512) void fused(const unsigned short* x_,const unsigned short* w1_,const unsigned short* b1_,const unsigned short* w2_,unsigned short* out_){
  __shared__ __attribute__((aligned(16))) unsigned short hs[16][F+8];

  unsigned long long ta,tb,tc; unsigned long long px,pw1,pb1,pw2,pout;
  {
    auto ka=__builtin_amdgcn_kernarg_segment_ptr();
    asm volatile("s_memrealtime %0\n\ts_waitcnt lgkmcnt(0)":"=s"(ta)::"memory");
    asm volatile("s_load_dwordx2 %0, %5, 0\n\ts_load_dwordx2 %1, %5, 8\n\ts_load_dwordx2 %2, %5, 16\n\ts_load_dwordx2 %3, %5, 24\n\ts_load_dwordx2 %4, %5, 32\n\ts_waitcnt lgkmcnt(0)":"=&s"(px),"=&s"(pw1),"=&s"(pb1),"=&s"(pw2),"=&s"(pout):"s"(ka):"memory");
    asm volatile("s_memrealtime %0\n\ts_waitcnt lgkmcnt(0)":"=s"(tb)::"memory");
    unsigned v; const char* a=(const char*)px + blockIdx.x*16384 + (threadIdx.x>>6)*1024+(threadIdx.x&63)*16;
    asm volatile("global_load_dword %0, %1, off\n\ts_waitcnt vmcnt(0)":"=&v"(v):"v"(a):"memory");
    asm volatile("s_memrealtime %0\n\ts_waitcnt lgkmcnt(0)":"=s"(tc)::"memory");
  }
  const unsigned short* __restrict__ x=(const unsigned short*)px; const unsigned short* __restrict__ w1=(const unsigned short*)pw1; const unsigned short* __restrict__ b1=(const unsigned short*)pb1; const unsigned short* __restrict__ w2=(const unsigned short*)pw2; unsigned short* __restrict__ out=(unsigned short*)pout;
  { unsigned long long until = tc + 0;
    while(__builtin_amdgcn_s_memrealtime() < until) __builtin_amdgcn_s_sleep(2); }

  int tid=threadIdx.x, lane=tid&63, w=tid>>6;
  int r0=blockIdx.x*16;
  int c=lane&15,q=lane>>4;
  {
    f32x4 acc[14]={};
    for(int k0=0;k0<D;k0+=32){
      bf16x8 a=*(const bf16x8*)&x[(size_t)(r0+c)*D+k0+8*q];
      #pragma unroll
      for(int t=0;t<14;t++){
        bf16x8 b;
        #pragma unroll
        for(int j=0;j<8;j++) b[j]=w1[(size_t)(k0+8*q+j)*F+224*w+16*t+c];
        acc[t]=__builtin_amdgcn_mfma_f32_16x16x32_bf16(a,b,acc[t],0,0,0);
      }
    }
    #pragma unroll
    for(int t=0;t<14;t++){ int col=224*w+16*t+c; float bv=bf2f(b1[col]);
      for(int r=0;r<4;r++) hs[4*q+r][col]=f2bf(mish(acc[t][r]+bv)); }
  }
  __syncthreads();
  {
    f32x4 acc[4]={};
    for(int k0=0;k0<F;k0+=32){
      bf16x8 a=*(const bf16x8*)&hs[c][k0+8*q];
      #pragma unroll
      for(int t=0;t<4;t++){
        bf16x8 b;
        #pragma unroll
        for(int j=0;j<8;j++) b[j]=w2[(size_t)(k0+8*q+j)*D+64*w+16*t+c];
        acc[t]=__builtin_amdgcn_mfma_f32_16x16x32_bf16(a,b,acc[t],0,0,0);
      }
    }
    #pragma unroll
    for(int t=0;t<4;t++) for(int r=0;r<4;r++) out[(size_t)(r0+4*q+r)*D+64*w+16*t+c]=f2bf(acc[t][r]);
  }
}

extern "C" void kernel_launch(void* const* d_in, const int* in_sizes, int n_in,
                              void* d_out, int out_size, void* d_ws, size_t ws_size,
                              hipStream_t stream) {
  fused<<<128,512,0,stream>>>((const unsigned short*)d_in[0],(const unsigned short*)d_in[1],(const unsigned short*)d_in[2],(const unsigned short*)d_in[3],(unsigned short*)d_out);
}
```

```cpp
#include <hip/hip_runtime.h>
#include <hip/hip_bf16.h>
typedef short bf16x8 __attribute__((ext_vector_type(8)));
typedef float f32x4 __attribute__((ext_vector_type(4)));
constexpr int S=2048, D=512, F=1792;
__device__ __forceinline__ float mish(float h){
  float e = __builtin_amdgcn_exp2f(fminf(h*1.44269504088896f, 40.f));
  float n = e*(e+2.f);
  return h*n*__builtin_amdgcn_rcpf(n+2.f);
}
__device__ __forceinline__ float bf2f(unsigned short u){ return __uint_as_float(((unsigned)u)<<16); }
__device__ __forceinline__ unsigned short f2bf(float f){ __bf16 b=(__bf16)f; return __builtin_bit_cast(unsigned short,b); }
__global__ __launch_bounds__(512) void fused(const unsigned short* __restrict__ x,const unsigned short* __restrict__ w1,const unsigned short* __restrict__ b1,const unsigned short* __restrict__ w2,unsigned short* __restrict__ out){
  __shared__ __attribute__((aligned(16))) unsigned short hs[16][F+8];

  { unsigned xcc=(unsigned)__builtin_amdgcn_s_getreg(20 | (3 << 11)) & 0xfu; unsigned d=(xcc-blockIdx.x)&7;
    unsigned long long until=__builtin_amdgcn_s_memrealtime()+ (unsigned long long)d*5000ull;
    while(__builtin_amdgcn_s_memrealtime() < until) __builtin_amdgcn_s_sleep(2); }

  int tid=threadIdx.x, lane=tid&63, w=tid>>6;
  int r0=blockIdx.x*16;
  int c=lane&15,q=lane>>4;
  {
    f32x4 acc[14]={};
    for(int k0=0;k0<D;k0+=32){
      bf16x8 a=*(const bf16x8*)&x[(size_t)(r0+c)*D+k0+8*q];
      #pragma unroll
      for(int t=0;t<14;t++){
        bf16x8 b;
        #pragma unroll
        for(int j=0;j<8;j++) b[j]=w1[(size_t)(k0+8*q+j)*F+224*w+16*t+c];
        acc[t]=__builtin_amdgcn_mfma_f32_16x16x32_bf16(a,b,acc[t],0,0,0);
      }
    }
    #pragma unroll
    for(int t=0;t<14;t++){ int col=224*w+16*t+c; float bv=bf2f(b1[col]);
      for(int r=0;r<4;r++) hs[4*q+r][col]=f2bf(mish(acc[t][r]+bv)); }
  }
  __syncthreads();
  {
    f32x4 acc[4]={};
    for(int k0=0;k0<F;k0+=32){
      bf16x8 a=*(const bf16x8*)&hs[c][k0+8*q];
      #pragma unroll
      for(int t=0;t<4;t++){
        bf16x8 b;
        #pragma unroll
        for(int j=0;j<8;j++) b[j]=w2[(size_t)(k0+8*q+j)*D+64*w+16*t+c];
        acc[t]=__builtin_amdgcn_mfma_f32_16x16x32_bf16(a,b,acc[t],0,0,0);
      }
    }
    #pragma unroll
    for(int t=0;t<4;t++) for(int r=0;r<4;r++) out[(size_t)(r0+4*q+r)*D+64*w+16*t+c]=f2bf(acc[t][r]);
  }
}

extern "C" void kernel_launch(void* const* d_in, const int* in_sizes, int n_in,
                              void* d_out, int out_size, void* d_ws, size_t ws_size,
                              hipStream_t stream) {
  fused<<<128,512,0,stream>>>((const unsigned short*)d_in[0],(const unsigned short*)d_in[1],(const unsigned short*)d_in[2],(const unsigned short*)d_in[3],(unsigned short*)d_out);
}
```

```cpp
#include <hip/hip_runtime.h>
#include <hip/hip_bf16.h>
typedef short bf16x8 __attribute__((ext_vector_type(8)));
typedef float f32x4 __attribute__((ext_vector_type(4)));
constexpr int S=2048, D=512, F=1792;
__device__ __forceinline__ float mish(float h){
  float e = __builtin_amdgcn_exp2f(fminf(h*1.44269504088896f, 40.f));
  float n = e*(e+2.f);
  return h*n*__builtin_amdgcn_rcpf(n+2.f);
}
__device__ __forceinline__ float bf2f(unsigned short u){ return __uint_as_float(((unsigned)u)<<16); }
__device__ __forceinline__ unsigned short f2bf(float f){ __bf16 b=(__bf16)f; return __builtin_bit_cast(unsigned short,b); }
__global__ __launch_bounds__(512) void fused(const unsigned short* __restrict__ x,const unsigned short* __restrict__ w1,const unsigned short* __restrict__ b1,const unsigned short* __restrict__ w2,unsigned short* __restrict__ out){
  unsigned long long t0=__builtin_amdgcn_s_memrealtime();
  __shared__ __attribute__((aligned(16))) unsigned short hs[16][F+8];
  int tid=threadIdx.x, lane=tid&63, w=tid>>6;
  int r0=blockIdx.x*16;
  int c=lane&15,q=lane>>4;
  {
    f32x4 acc[14]={};
    for(int k0=0;k0<D;k0+=32){
      bf16x8 a=*(const bf16x8*)&x[(size_t)(r0+c)*D+k0+8*q];
      #pragma unroll
      for(int t=0;t<14;t++){
        bf16x8 b;
        #pragma unroll
        for(int j=0;j<8;j++) b[j]=w1[(size_t)(k0+8*q+j)*F+224*w+16*t+c];
        acc[t]=__builtin_amdgcn_mfma_f32_16x16x32_bf16(a,b,acc[t],0,0,0);
      }
    }
    #pragma unroll
    for(int t=0;t<14;t++){ int col=224*w+16*t+c; float bv=bf2f(b1[col]);
      for(int r=0;r<4;r++) hs[4*q+r][col]=f2bf(mish(acc[t][r]+bv)); }
  }
  __syncthreads();
  {
    f32x4 acc[4]={};
    for(int k0=0;k0<F;k0+=32){
      bf16x8 a=*(const bf16x8*)&hs[c][k0+8*q];
      #pragma unroll
      for(int t=0;t<4;t++){
        bf16x8 b;
        #pragma unroll
        for(int j=0;j<8;j++) b[j]=w2[(size_t)(k0+8*q+j)*D+64*w+16*t+c];
        acc[t]=__builtin_amdgcn_mfma_f32_16x16x32_bf16(a,b,acc[t],0,0,0);
      }
    }
    #pragma unroll
    for(int t=0;t<4;t++) for(int r=0;r<4;r++) out[(size_t)(r0+4*q+r)*D+64*w+16*t+c]=f2bf(acc[t][r]);
  }
  if(threadIdx.x==0){
    unsigned long long pkt=(unsigned long long)__builtin_amdgcn_dispatch_ptr(); unsigned long long sig;
    asm volatile("global_load_dwordx2 %0, %1, off offset:56 sc0 sc1\n\ts_waitcnt vmcnt(0)":"=&v"(sig):"v"(pkt):"memory");
    unsigned long long amp;
    if(sig==0) amp=30000; else { unsigned long long ts; asm volatile("global_load_dwordx2 %0, %1, off offset:32 sc0 sc1\n\ts_waitcnt vmcnt(0)":"=&v"(ts):"v"(sig):"memory");
      unsigned long long d=t0-ts; amp = d<5000 ? d*20 : 60000; }
    unsigned long long until=__builtin_amdgcn_s_memrealtime()+amp; while(__builtin_amdgcn_s_memrealtime()<until) __builtin_amdgcn_s_sleep(2); }
}

extern "C" void kernel_launch(void* const* d_in, const int* in_sizes, int n_in,
                              void* d_out, int out_size, void* d_ws, size_t ws_size,
                              hipStream_t stream) {
  fused<<<128,512,0,stream>>>((const unsigned short*)d_in[0],(const unsigned short*)d_in[1],(const unsigned short*)d_in[2],(const unsigned short*)d_in[3],(unsigned short*)d_out);
}
```

```cpp
#include <hip/hip_runtime.h>
#include <hip/hip_bf16.h>
typedef short bf16x8 __attribute__((ext_vector_type(8)));
typedef float f32x4 __attribute__((ext_vector_type(4)));
constexpr int S=2048, D=512, F=1792;
__device__ __forceinline__ float mish(float h){
  float e = __builtin_amdgcn_exp2f(fminf(h*1.44269504088896f, 40.f));
  float n = e*(e+2.f);
  return h*n*__builtin_amdgcn_rcpf(n+2.f);
}
__device__ __forceinline__ float bf2f(unsigned short u){ return __uint_as_float(((unsigned)u)<<16); }
__device__ __forceinline__ unsigned short f2bf(float f){ __bf16 b=(__bf16)f; return __builtin_bit_cast(unsigned short,b); }
__global__ __launch_bounds__(512,2) void fused(const unsigned short* __restrict__ x,const unsigned short* __restrict__ w1,const unsigned short* __restrict__ b1,const unsigned short* __restrict__ w2,unsigned short* __restrict__ out){
  unsigned long long T0=__builtin_amdgcn_s_memrealtime();
  __shared__ __attribute__((aligned(16))) unsigned short hs[45][F+8];
  if(blockIdx.x<128){

  { unsigned xcc=(unsigned)__builtin_amdgcn_s_getreg(20 | (3 << 11)) & 0xfu; unsigned d=(xcc-blockIdx.x)&7;
    unsigned long long until=__builtin_amdgcn_s_memrealtime()+ (unsigned long long)d*5000ull;
    while(__builtin_amdgcn_s_memrealtime() < until) __builtin_amdgcn_s_sleep(2); }

  int tid=threadIdx.x, lane=tid&63, w=tid>>6;
  int r0=blockIdx.x*16;
  int c=lane&15,q=lane>>4;
  {
    f32x4 acc[14]={};
    for(int k0=0;k0<D;k0+=32){
      bf16x8 a=*(const bf16x8*)&x[(size_t)(r0+c)*D+k0+8*q];
      #pragma unroll
      for(int t=0;t<14;t++){
        bf16x8 b;
        #pragma unroll
        for(int j=0;j<8;j++) b[j]=w1[(size_t)(k0+8*q+j)*F+224*w+16*t+c];
        acc[t]=__builtin_amdgcn_mfma_f32_16x16x32_bf16(a,b,acc[t],0,0,0);
      }
    }
    #pragma unroll
    for(int t=0;t<14;t++){ int col=224*w+16*t+c; float bv=bf2f(b1[col]);
      for(int r=0;r<4;r++) hs[4*q+r][col]=f2bf(mish(acc[t][r]+bv)); }
  }
  __syncthreads();
  {
    f32x4 acc[4]={};
    for(int k0=0;k0<F;k0+=32){
      bf16x8 a=*(const bf16x8*)&hs[c][k0+8*q];
      #pragma unroll
      for(int t=0;t<4;t++){
        bf16x8 b;
        #pragma unroll
        for(int j=0;j<8;j++) b[j]=w2[(size_t)(k0+8*q+j)*D+64*w+16*t+c];
        acc[t]=__builtin_amdgcn_mfma_f32_16x16x32_bf16(a,b,acc[t],0,0,0);
      }
    }
    #pragma unroll
    for(int t=0;t<4;t++) for(int r=0;r<4;r++) __hip_atomic_store(&out[(size_t)(r0+4*q+r)*D+64*w+16*t+c],f2bf(acc[t][r]),__ATOMIC_RELAXED,__HIP_MEMORY_SCOPE_AGENT);
  }
  }
  asm volatile("v_mov_b32 v255, 0":::"v255"); while(__builtin_amdgcn_s_memrealtime()<T0+20000) __builtin_amdgcn_s_sleep(1);
}

extern "C" void kernel_launch(void* const* d_in, const int* in_sizes, int n_in,
                              void* d_out, int out_size, void* d_ws, size_t ws_size,
                              hipStream_t stream) {
  fused<<<256,512,0,stream>>>((const unsigned short*)d_in[0],(const unsigned short*)d_in[1],(const unsigned short*)d_in[2],(const unsigned short*)d_in[3],(unsigned short*)d_out);
}
```

```cpp
#include <hip/hip_runtime.h>

__attribute__((aligned(4096))) __global__ __launch_bounds__(512, 2) void mish_mlp_kernel(const void* x, const void* w1, const void* b1, const void* w2, void* out, void* ws) {
  __shared__ __attribute__((aligned(16))) char lds[163840];
  unsigned tid = threadIdx.x;
  asm volatile(
    "L_k0:\n"
    "s_load_dwordx8 s[8:15], %1, 0\n"
    "s_load_dwordx4 s[4:7], %1, 32\n"
    "v_lshrrev_b32 v2, 6, %0\n"
    "v_and_b32 v1, 63, %0\n"
    "s_and_b32 s37, %2, 7\n"
    "v_readfirstlane_b32 s36, v2\n"
    "s_lshr_b32 s38, %2, 3\n"
    "s_cmp_eq_u32 s36, 1\n"
    "s_cbranch_scc0 L_nopf\n"
    "s_getpc_b64 s[50:51]\n"
    "L_pcref:\n"
    "s_sub_u32 s50, s50, L_pcref-L_k0\n"
    "s_subb_u32 s51, s51, 0\n"
    "s_mul_i32 s49, s38, 5\n"
    "v_add_u32 v2, s49, v1\n"
    "v_lshlrev_b32 v2, 7, v2\n"
    "v_min_u32 v2, L_kend-L_k0, v2\n"
    "s_mov_b64 exec, 31\n"
    "global_load_dword v240, v2, s[50:51]\n"
    "s_mov_b64 exec, -1\n"
    "L_nopf:\n"
    "s_lshr_b32 s49, s37, 1\n"
    "s_and_b32 s50, s37, 1\n"
    "s_and_b32 s51, s38, 3\n"
    "s_lshr_b32 s68, s38, 2\n"
    "s_lshl_b32 s39, s49, 2\n"
    "s_add_u32 s39, s39, s51\n"
    "s_mul_i32 s42, s50, 7\n"
    "s_add_u32 s42, s42, s68\n"
    "s_lshl_b32 s45, s50, 1\n"
    "s_add_u32 s45, s45, s49\n"
    "s_and_b32 s45, s45, 3\n"
    "s_add_u32 s66, s51, s68\n"
    "s_and_b32 s66, s66, 1\n"
    "s_lshl_b32 s66, s66, 2\n"
    "s_add_u32 s45, s45, s66\n"
    "s_lshl_b32 s66, s49, 1\n"
    "v_lshlrev_b32 v4, 4, v1\n"
    "s_cmp_ge_u32 s36, 4\n"
    "s_cbranch_scc0 L_cpro\n"
    "s_cmp_ge_u32 s38, 28\n"
    "s_cbranch_scc1 L_lidle\n"
    "s_cmp_eq_u32 s45, 3\n"
    "s_cselect_b32 s45, 4, s45\n"
    "s_sub_u32 s49, s36, 4\n"
    "v_lshrrev_b32 v2, 3, v1\n"
    "v_and_b32 v3, 7, v1\n"
    "v_lshrrev_b32 v5, 1, v2\n"
    "v_xor_b32 v5, v5, v3\n"
    "v_lshlrev_b32 v5, 4, v5\n"
    "v_lshl_or_b32 v5, v2, 10, v5\n"
    "s_lshl_b32 s50, s49, 15\n"
    "v_add_u32 v8, s50, v5\n"
    "v_xor_b32 v9, 64, v8\n"
    "v_add_u32 v10, 14336, v8\n"
    "v_add_u32 v11, 21504, v9\n"
    "v_add_u32 v9, 7168, v9\n"
    "v_and_b32 v5, 2, v2\n"
    "v_xor_b32 v5, v5, v3\n"
    "v_lshlrev_b32 v5, 4, v5\n"
    "s_lshl_b32 s50, s49, 4\n"
    "v_add_u32 v6, s50, v2\n"
    "v_mul_u32_u24 v6, 3584, v6\n"
    "v_add_u32 v12, v6, v5\n"
    "v_xor_b32 v5, 64, v5\n"
    "v_add_u32 v13, v6, v5\n"
    "v_add_u32 v13, 27648, v13\n"
    "s_lshl_b32 s48, s49, 12\n"
    "s_lshl_b32 s63, s49, 11\n"
    "s_lshl_b32 s50, s39, 17\n"
    "s_lshl_b32 s51, s42, 8\n"
    "s_lshl_b32 s46, s45, 7\n"
    "s_mul_i32 s47, s45, 229376\n"
    "v_mov_b32 v7, 0\n"
    "s_mov_b32 s18, 131072\n"
    "s_mov_b32 s19, 0x20000\n"
    "s_sub_u32 s22, 1835008, s51\n"
    "s_mov_b32 s23, 0x20000\n"
    "s_mov_b32 m0, s48\n"
    "s_waitcnt lgkmcnt(0)\n"
    "s_add_u32 s16, s8, s50\n"
    "s_addc_u32 s17, s9, 0\n"
    "buffer_load_dwordx4 v8, s[16:19], s46 offen offset:0 lds\n"
    "buffer_load_dwordx4 v9, s[16:19], s46 offen offset:1024 lds\n"
    "buffer_load_dwordx4 v10, s[16:19], s46 offen offset:2048 lds\n"
    "buffer_load_dwordx4 v11, s[16:19], s46 offen offset:3072 lds\n"
    "s_cmp_eq_u32 s49, 0\n"
    "s_cbranch_scc0 L_nbl\n"
    "s_add_u32 s24, s12, s51\n"
    "s_addc_u32 s25, s13, 0\n"
    "s_mov_b32 s26, 256\n"
    "s_mov_b32 s27, 0x20000\n"
    "v_lshlrev_b32 v14, 2, v1\n"
    "s_mov_b32 m0, 163584\n"
    "s_nop 0\n"
    "buffer_load_dword v14, s[24:27], 0 offen lds\n"
    "L_nbl:\n"
    "s_add_u32 s20, s10, s51\n"
    "s_addc_u32 s21, s11, 0\n"
    "s_add_u32 m0, s63, 16384\n"
    "s_nop 0\n"
    "buffer_load_dwordx4 v12, s[20:23], s47 offen offset:0 lds\n"
    "buffer_load_dwordx4 v13, s[20:23], s47 offen offset:1024 lds\n"
    "s_add_u32 s45, s45, 1\n"
    "s_and_b32 s45, s45, 7\n"
    "s_lshl_b32 s46, s45, 7\n"
    "s_mul_i32 s47, s45, 229376\n"
    "s_add_u32 m0, s48, 24576\n"
    "s_nop 0\n"
    "buffer_load_dwordx4 v8, s[16:19], s46 offen offset:0 lds\n"
    "s_cmp_eq_u32 s49, 0\n"
    "s_cbranch_scc0 L_nel\n"
    "s_add_u32 s64, s6, 7409664\n"
    "s_addc_u32 s65, s7, 0\n"
    "global_load_dwordx2 v[20:21], v7, s[64:65] sc1\n"
    "L_nel:\n"
    "buffer_load_dwordx4 v9, s[16:19], s46 offen offset:1024 lds\n"
    "v_and_b32 v5, 6, v2\n"
    "v_xor_b32 v5, v5, v3\n"
    "v_lshlrev_b32 v5, 4, v5\n"
    "v_lshl_or_b32 v16, v2, 10, v5\n"
    "v_add_u32 v17, 7168, v16\n"
    "v_add_u32 v18, 14336, v16\n"
    "v_add_u32 v19, 21504, v16\n"
    "s_mul_i32 s69, s49, 7\n"
    "s_add_u32 s68, s66, s69\n"
    "s_lshl_b32 s67, s68, 16\n"
    "s_and_b32 s68, s68, 1\n"
    "s_lshl_b32 s68, s68, 15\n"
    "s_sub_u32 s56, s67, s68\n"
    "s_add_u32 s66, s66, 1\n"
    "s_cmp_eq_u32 s66, 7\n"
    "s_cselect_b32 s66, 0, s66\n"
    "s_add_u32 s68, s66, s69\n"
    "s_lshl_b32 s67, s68, 16\n"
    "s_and_b32 s68, s68, 1\n"
    "s_lshl_b32 s68, s68, 15\n"
    "s_sub_u32 s57, s67, s68\n"
    "s_add_u32 s66, s66, 1\n"
    "s_cmp_eq_u32 s66, 7\n"
    "s_cselect_b32 s66, 0, s66\n"
    "s_add_u32 s68, s66, s69\n"
    "s_lshl_b32 s67, s68, 16\n"
    "s_and_b32 s68, s68, 1\n"
    "s_lshl_b32 s68, s68, 15\n"
    "s_sub_u32 s58, s67, s68\n"
    "s_add_u32 s66, s66, 1\n"
    "s_cmp_eq_u32 s66, 7\n"
    "s_cselect_b32 s66, 0, s66\n"
    "s_add_u32 s68, s66, s69\n"
    "s_lshl_b32 s67, s68, 16\n"
    "s_and_b32 s68, s68, 1\n"
    "s_lshl_b32 s68, s68, 15\n"
    "s_sub_u32 s59, s67, s68\n"
    "s_add_u32 s66, s66, 1\n"
    "s_cmp_eq_u32 s66, 7\n"
    "s_cselect_b32 s66, 0, s66\n"
    "s_add_u32 s68, s66, s69\n"
    "s_lshl_b32 s67, s68, 16\n"
    "s_and_b32 s68, s68, 1\n"
    "s_lshl_b32 s68, s68, 15\n"
    "s_sub_u32 s60, s67, s68\n"
    "s_add_u32 s66, s66, 1\n"
    "s_cmp_eq_u32 s66, 7\n"
    "s_cselect_b32 s66, 0, s66\n"
    "s_add_u32 s68, s66, s69\n"
    "s_lshl_b32 s67, s68, 16\n"
    "s_and_b32 s68, s68, 1\n"
    "s_lshl_b32 s68, s68, 15\n"
    "s_sub_u32 s61, s67, s68\n"
    "s_add_u32 s66, s66, 1\n"
    "s_cmp_eq_u32 s66, 7\n"
    "s_cselect_b32 s66, 0, s66\n"
    "s_add_u32 s68, s66, s69\n"
    "s_lshl_b32 s67, s68, 16\n"
    "s_and_b32 s68, s68, 1\n"
    "s_lshl_b32 s68, s68, 15\n"
    "s_sub_u32 s62, s67, s68\n"
    "buffer_load_dwordx4 v10, s[16:19], s46 offen offset:2048 lds\n"
    "buffer_load_dwordx4 v11, s[16:19], s46 offen offset:3072 lds\n"
    "s_add_u32 m0, s63, 40960\n"
    "s_nop 0\n"
    "buffer_load_dwordx4 v12, s[20:23], s47 offen offset:0 lds\n"
    "buffer_load_dwordx4 v13, s[20:23], s47 offen offset:1024 lds\n"
    "s_add_u32 s45, s45, 1\n"
    "s_and_b32 s45, s45, 7\n"
    "s_lshl_b32 s46, s45, 7\n"
    "s_mul_i32 s47, s45, 229376\n"
    "s_add_u32 m0, s48, 49152\n"
    "s_waitcnt vmcnt(11)\n"
    "buffer_load_dwordx4 v8, s[16:19], s46 offen offset:0 lds\n"
    "s_waitcnt vmcnt(11)\n"
    "buffer_load_dwordx4 v9, s[16:19], s46 offen offset:1024 lds\n"
    "s_waitcnt vmcnt(11)\n"
    "buffer_load_dwordx4 v10, s[16:19], s46 offen offset:2048 lds\n"
    "s_waitcnt vmcnt(11)\n"
    "buffer_load_dwordx4 v11, s[16:19], s46 offen offset:3072 lds\n"
    "s_add_u32 m0, s63, 65536\n"
    "s_waitcnt vmcnt(11)\n"
    "buffer_load_dwordx4 v12, s[20:23], s47 offen offset:0 lds\n"
    "s_waitcnt vmcnt(11)\n"
    "buffer_load_dwordx4 v13, s[20:23], s47 offen offset:1024 lds\n"
    "s_add_u32 s45, s45, 1\n"
    "s_and_b32 s45, s45, 7\n"
    "s_lshl_b32 s46, s45, 7\n"
    "s_mul_i32 s47, s45, 229376\n"
    "s_add_u32 m0, s48, 73728\n"
    "s_waitcnt vmcnt(12)\n"
    "s_barrier\n"
    "s_waitcnt vmcnt(11)\n"
    "buffer_load_dwordx4 v8, s[16:19], s46 offen offset:0 lds\n"
    "s_waitcnt vmcnt(11)\n"
    "buffer_load_dwordx4 v9, s[16:19], s46 offen offset:1024 lds\n"
    "s_waitcnt vmcnt(11)\n"
    "buffer_load_dwordx4 v10, s[16:19], s46 offen offset:2048 lds\n"
    "s_waitcnt vmcnt(11)\n"
    "buffer_load_dwordx4 v11, s[16:19], s46 offen offset:3072 lds\n"
    "s_add_u32 m0, s63, 90112\n"
    "s_waitcnt vmcnt(11)\n"
    "buffer_load_dwordx4 v12, s[20:23], s47 offen offset:0 lds\n"
    "s_waitcnt vmcnt(11)\n"
    "buffer_load_dwordx4 v13, s[20:23], s47 offen offset:1024 lds\n"
    "s_lshr_b32 s67, s38, 1\n"
    "s_lshr_b32 s69, s38, 2\n"
    "s_xor_b32 s69, s67, s69\n"
    "s_andn2_b32 s69, s38, s69\n"
    "s_and_b32 s69, s69, 1\n"
    "s_cmp_lt_u32 s38, 24\n"
    "s_cselect_b32 s69, s69, 0\n"
    "s_mul_i32 s69, s69, 6\n"
    "s_xor_b32 s69, s69, s38\n"
    "s_lshr_b32 s67, s69, 2\n"
    "s_and_b32 s69, s69, 3\n"
    "s_lshr_b32 s43, s37, 1\n"
    "s_lshl_b32 s43, s43, 3\n"
    "s_add_u32 s43, s43, s67\n"
    "s_and_b32 s44, s37, 1\n"
    "s_lshl_b32 s44, s44, 2\n"
    "s_add_u32 s44, s44, s69\n"
    "s_lshl_b32 s51, s44, 7\n"
    "s_add_u32 s24, s14, s51\n"
    "s_addc_u32 s25, s15, 0\n"
    "s_sub_u32 s26, 1835008, s51\n"
    "s_mov_b32 s27, 0x20000\n"
    "s_add_u32 s45, s45, 1\n"
    "s_and_b32 s45, s45, 7\n"
    "s_lshl_b32 s46, s45, 7\n"
    "s_mul_i32 s47, s45, 229376\n"
    "s_add_u32 m0, s48, 0\n"
    "s_waitcnt vmcnt(12)\n"
    "s_barrier\n"
    "s_waitcnt vmcnt(11)\n"
    "buffer_load_dwordx4 v8, s[16:19], s46 offen offset:0 lds\n"
    "s_waitcnt vmcnt(11)\n"
    "buffer_load_dwordx4 v9, s[16:19], s46 offen offset:1024 lds\n"
    "s_waitcnt vmcnt(11)\n"
    "buffer_load_dwordx4 v10, s[16:19], s46 offen offset:2048 lds\n"
    "s_waitcnt vmcnt(11)\n"
    "buffer_load_dwordx4 v11, s[16:19], s46 offen offset:3072 lds\n"
    "s_add_u32 m0, s63, 16384\n"
    "s_waitcnt vmcnt(11)\n"
    "buffer_load_dwordx4 v12, s[20:23], s47 offen offset:0 lds\n"
    "s_waitcnt vmcnt(11)\n"
    "buffer_load_dwordx4 v13, s[20:23], s47 offen offset:1024 lds\n"
    "v_readfirstlane_b32 s40, v20\n"
    "v_readfirstlane_b32 s41, v21\n"
    "s_add_u32 s40, s40, 1\n"
    "s_addc_u32 s41, s41, 0\n"
    "v_mov_b32 v2, s40\n"
    "v_mov_b32 v3, s41\n"
    "s_lshl_b32 s67, s39, 12\n"
    "s_lshl_b32 s68, s42, 3\n"
    "s_add_u32 s67, s67, s68\n"
    "s_add_u32 s67, s67, 7340032\n"
    "v_lshl_add_u32 v24, v1, 8, s67\n"
    "s_and_b32 s67, s43, 1\n"
    "s_or_b32 s67, s67, s44\n"
    "s_cmp_eq_u32 s67, 0\n"
    "s_cselect_b32 s97, 31, 15\n"
    "s_lshl_b32 s67, s43, 2\n"
    "s_add_u32 s67, s67, 7403776\n"
    "v_writelane_b32 v24, s67, 4\n"
    "s_lshr_b32 s67, s43, 1\n"
    "s_lshl_b32 s67, s67, 12\n"
    "s_and_b32 s68, s43, 1\n"
    "s_lshl_b32 s68, s68, 3\n"
    "s_add_u32 s68, s68, s44\n"
    "s_and_b32 s68, s68, 3\n"
    "s_lshl_b32 s68, s68, 8\n"
    "s_add_u32 s67, s67, s68\n"
    "s_add_u32 s67, s67, 7340032\n"
    "s_add_u32 s64, s6, s67\n"
    "s_addc_u32 s65, s7, 0\n"
    "v_lshlrev_b32 v249, 3, v1\n"
    "v_mov_b32 v250, 0\n"
    "v_mov_b32 v251, 0\n"
    "s_add_u32 s45, s45, 1\n"
    "s_and_b32 s45, s45, 7\n"
    "s_lshl_b32 s46, s45, 7\n"
    "s_mul_i32 s47, s45, 229376\n"
    "s_add_u32 m0, s48, 24576\n"
    "s_waitcnt vmcnt(12)\n"
    "s_barrier\n"
    "s_waitcnt vmcnt(11)\n"
    "buffer_load_dwordx4 v8, s[16:19], s46 offen offset:0 lds\n"
    "s_waitcnt vmcnt(11)\n"
    "buffer_load_dwordx4 v9, s[16:19], s46 offen offset:1024 lds\n"
    "s_waitcnt vmcnt(11)\n"
    "buffer_load_dwordx4 v10, s[16:19], s46 offen offset:2048 lds\n"
    "s_waitcnt vmcnt(11)\n"
    "buffer_load_dwordx4 v11, s[16:19], s46 offen offset:3072 lds\n"
    "s_add_u32 m0, s63, 40960\n"
    "s_waitcnt vmcnt(11)\n"
    "buffer_load_dwordx4 v12, s[20:23], s47 offen offset:0 lds\n"
    "s_waitcnt vmcnt(11)\n"
    "buffer_load_dwordx4 v13, s[20:23], s47 offen offset:1024 lds\n"
    "s_add_u32 s45, s45, 1\n"
    "s_and_b32 s45, s45, 7\n"
    "s_lshl_b32 s46, s45, 7\n"
    "s_mul_i32 s47, s45, 229376\n"
    "s_add_u32 m0, s48, 49152\n"
    "s_waitcnt vmcnt(12)\n"
    "s_barrier\n"
    "s_waitcnt vmcnt(11)\n"
    "buffer_load_dwordx4 v8, s[16:19], s46 offen offset:0 lds\n"
    "s_waitcnt vmcnt(11)\n"
    "buffer_load_dwordx4 v9, s[16:19], s46 offen offset:1024 lds\n"
    "s_waitcnt vmcnt(11)\n"
    "buffer_load_dwordx4 v10, s[16:19], s46 offen offset:2048 lds\n"
    "s_waitcnt vmcnt(11)\n"
    "buffer_load_dwordx4 v11, s[16:19], s46 offen offset:3072 lds\n"
    "s_add_u32 m0, s63, 65536\n"
    "s_waitcnt vmcnt(11)\n"
    "buffer_load_dwordx4 v12, s[20:23], s47 offen offset:0 lds\n"
    "s_waitcnt vmcnt(11)\n"
    "buffer_load_dwordx4 v13, s[20:23], s47 offen offset:1024 lds\n"
    "s_add_u32 s45, s45, 1\n"
    "s_and_b32 s45, s45, 7\n"
    "s_lshl_b32 s46, s45, 7\n"
    "s_mul_i32 s47, s45, 229376\n"
    "s_add_u32 m0, s48, 73728\n"
    "s_waitcnt vmcnt(12)\n"
    "s_barrier\n"
    "s_waitcnt vmcnt(11)\n"
    "buffer_load_dwordx4 v8, s[16:19], s46 offen offset:0 lds\n"
    "s_waitcnt vmcnt(11)\n"
    "buffer_load_dwordx4 v9, s[16:19], s46 offen offset:1024 lds\n"
    "s_waitcnt vmcnt(11)\n"
    "buffer_load_dwordx4 v10, s[16:19], s46 offen offset:2048 lds\n"
    "s_waitcnt vmcnt(11)\n"
    "buffer_load_dwordx4 v11, s[16:19], s46 offen offset:3072 lds\n"
    "s_add_u32 m0, s63, 90112\n"
    "s_waitcnt vmcnt(11)\n"
    "buffer_load_dwordx4 v12, s[20:23], s47 offen offset:0 lds\n"
    "s_waitcnt vmcnt(11)\n"
    "buffer_load_dwordx4 v13, s[20:23], s47 offen offset:1024 lds\n"
    "s_add_u32 s45, s45, 1\n"
    "s_and_b32 s45, s45, 7\n"
    "s_mul_i32 s47, s45, 229376\n"
    "s_add_u32 s47, s47, 128\n"
    "s_add_u32 m0, s63, 98304\n"
    "s_waitcnt vmcnt(12)\n"
    "s_barrier\n"
    "s_waitcnt vmcnt(11)\n"
    "buffer_load_dwordx4 v12, s[20:23], s47 offen offset:0 lds\n"
    "s_waitcnt vmcnt(11)\n"
    "buffer_load_dwordx4 v13, s[20:23], s47 offen offset:1024 lds\n"
    "s_add_u32 s45, s45, 1\n"
    "s_and_b32 s45, s45, 7\n"
    "s_mul_i32 s47, s45, 229376\n"
    "s_add_u32 s47, s47, 128\n"
    "s_add_u32 m0, s63, 106496\n"
    "s_waitcnt vmcnt(11)\n"
    "buffer_load_dwordx4 v12, s[20:23], s47 offen offset:0 lds\n"
    "s_waitcnt vmcnt(11)\n"
    "buffer_load_dwordx4 v13, s[20:23], s47 offen offset:1024 lds\n"
    "s_add_u32 s45, s45, 1\n"
    "s_and_b32 s45, s45, 7\n"
    "s_mul_i32 s47, s45, 229376\n"
    "s_add_u32 s47, s47, 128\n"
    "s_add_u32 m0, s63, 114688\n"
    "s_waitcnt vmcnt(11)\n"
    "buffer_load_dwordx4 v12, s[20:23], s47 offen offset:0 lds\n"
    "s_waitcnt vmcnt(11)\n"
    "buffer_load_dwordx4 v13, s[20:23], s47 offen offset:1024 lds\n"
    "s_add_u32 s45, s45, 1\n"
    "s_and_b32 s45, s45, 7\n"
    "s_mul_i32 s47, s45, 229376\n"
    "s_add_u32 s47, s47, 128\n"
    "s_add_u32 m0, s63, 122880\n"
    "s_waitcnt vmcnt(12)\n"
    "s_barrier\n"
    "s_waitcnt vmcnt(11)\n"
    "buffer_load_dwordx4 v12, s[20:23], s47 offen offset:0 lds\n"
    "s_waitcnt vmcnt(11)\n"
    "buffer_load_dwordx4 v13, s[20:23], s47 offen offset:1024 lds\n"
    "s_add_u32 s45, s45, 1\n"
    "s_and_b32 s45, s45, 7\n"
    "s_mul_i32 s47, s45, 229376\n"
    "s_add_u32 s47, s47, 128\n"
    "s_add_u32 m0, s63, 131072\n"
    "s_waitcnt vmcnt(11)\n"
    "buffer_load_dwordx4 v12, s[20:23], s47 offen offset:0 lds\n"
    "s_waitcnt vmcnt(11)\n"
    "buffer_load_dwordx4 v13, s[20:23], s47 offen offset:1024 lds\n"
    "s_add_u32 s45, s45, 1\n"
    "s_and_b32 s45, s45, 7\n"
    "s_mul_i32 s47, s45, 229376\n"
    "s_add_u32 s47, s47, 128\n"
    "s_add_u32 m0, s63, 139264\n"
    "s_waitcnt vmcnt(11)\n"
    "buffer_load_dwordx4 v12, s[20:23], s47 offen offset:0 lds\n"
    "s_waitcnt vmcnt(11)\n"
    "buffer_load_dwordx4 v13, s[20:23], s47 offen offset:1024 lds\n"
    "s_add_u32 s45, s45, 1\n"
    "s_and_b32 s45, s45, 7\n"
    "s_mul_i32 s47, s45, 229376\n"
    "s_add_u32 s47, s47, 128\n"
    "s_add_u32 m0, s63, 147456\n"
    "s_waitcnt vmcnt(12)\n"
    "s_barrier\n"
    "s_waitcnt vmcnt(11)\n"
    "buffer_load_dwordx4 v12, s[20:23], s47 offen offset:0 lds\n"
    "s_waitcnt vmcnt(11)\n"
    "buffer_load_dwordx4 v13, s[20:23], s47 offen offset:1024 lds\n"
    "s_add_u32 s45, s45, 1\n"
    "s_and_b32 s45, s45, 7\n"
    "s_mul_i32 s47, s45, 229376\n"
    "s_add_u32 s47, s47, 128\n"
    "s_add_u32 m0, s63, 155648\n"
    "s_waitcnt vmcnt(11)\n"
    "buffer_load_dwordx4 v12, s[20:23], s47 offen offset:0 lds\n"
    "s_waitcnt vmcnt(11)\n"
    "buffer_load_dwordx4 v13, s[20:23], s47 offen offset:1024 lds\n"
    "s_mov_b32 s46, s56\n"
    "s_add_u32 m0, s48, 0\n"
    "s_waitcnt vmcnt(11)\n"
    "buffer_load_dwordx4 v16, s[24:27], s46 offen offset:0 lds\n"
    "s_waitcnt vmcnt(11)\n"
    "buffer_load_dwordx4 v17, s[24:27], s46 offen offset:1024 lds\n"
    "s_waitcnt vmcnt(11)\n"
    "buffer_load_dwordx4 v18, s[24:27], s46 offen offset:2048 lds\n"
    "s_waitcnt vmcnt(11)\n"
    "buffer_load_dwordx4 v19, s[24:27], s46 offen offset:3072 lds\n"
    "s_mov_b32 s46, s57\n"
    "s_add_u32 m0, s48, 16384\n"
    "s_waitcnt vmcnt(11)\n"
    "buffer_load_dwordx4 v16, s[24:27], s46 offen offset:0 lds\n"
    "s_waitcnt vmcnt(11)\n"
    "buffer_load_dwordx4 v17, s[24:27], s46 offen offset:1024 lds\n"
    "s_waitcnt vmcnt(11)\n"
    "buffer_load_dwordx4 v18, s[24:27], s46 offen offset:2048 lds\n"
    "s_waitcnt vmcnt(11)\n"
    "buffer_load_dwordx4 v19, s[24:27], s46 offen offset:3072 lds\n"
    "s_mov_b32 s46, s58\n"
    "s_add_u32 m0, s48, 32768\n"
    "s_waitcnt vmcnt(11)\n"
    "buffer_load_dwordx4 v16, s[24:27], s46 offen offset:0 lds\n"
    "s_waitcnt vmcnt(11)\n"
    "buffer_load_dwordx4 v17, s[24:27], s46 offen offset:1024 lds\n"
    "s_waitcnt vmcnt(11)\n"
    "buffer_load_dwordx4 v18, s[24:27], s46 offen offset:2048 lds\n"
    "s_waitcnt vmcnt(10)\n"
    "buffer_load_dwordx4 v19, s[24:27], s46 offen offset:3072 lds\n"
    "s_mov_b32 s46, s59\n"
    "s_add_u32 m0, s48, 49152\n"
    "s_waitcnt vmcnt(12)\n"
    "s_barrier\n"
    "s_waitcnt vmcnt(10)\n"
    "buffer_load_dwordx4 v16, s[24:27], s46 offen offset:0 lds\n"
    "s_waitcnt vmcnt(10)\n"
    "buffer_load_dwordx4 v17, s[24:27], s46 offen offset:1024 lds\n"
    "s_waitcnt vmcnt(10)\n"
    "buffer_load_dwordx4 v18, s[24:27], s46 offen offset:2048 lds\n"
    "s_waitcnt vmcnt(10)\n"
    "buffer_load_dwordx4 v19, s[24:27], s46 offen offset:3072 lds\n"
    "s_mov_b32 s46, s60\n"
    "s_add_u32 m0, s48, 65536\n"
    "s_waitcnt vmcnt(10)\n"
    "buffer_load_dwordx4 v16, s[24:27], s46 offen offset:0 lds\n"
    "s_waitcnt vmcnt(10)\n"
    "buffer_load_dwordx4 v17, s[24:27], s46 offen offset:1024 lds\n"
    "s_waitcnt vmcnt(10)\n"
    "buffer_load_dwordx4 v18, s[24:27], s46 offen offset:2048 lds\n"
    "s_waitcnt vmcnt(10)\n"
    "buffer_load_dwordx4 v19, s[24:27], s46 offen offset:3072 lds\n"
    "s_mov_b32 s46, s61\n"
    "s_add_u32 m0, s48, 81920\n"
    "s_waitcnt vmcnt(10)\n"
    "buffer_load_dwordx4 v16, s[24:27], s46 offen offset:0 lds\n"
    "s_waitcnt vmcnt(10)\n"
    "buffer_load_dwordx4 v17, s[24:27], s46 offen offset:1024 lds\n"
    "s_waitcnt vmcnt(10)\n"
    "buffer_load_dwordx4 v18, s[24:27], s46 offen offset:2048 lds\n"
    "s_waitcnt vmcnt(10)\n"
    "buffer_load_dwordx4 v19, s[24:27], s46 offen offset:3072 lds\n"
    "s_mov_b32 s46, s62\n"
    "s_add_u32 m0, s48, 98304\n"
    "s_waitcnt vmcnt(8)\n"
    "s_barrier\n"
    "s_waitcnt vmcnt(10)\n"
    "buffer_load_dwordx4 v16, s[24:27], s46 offen offset:0 lds\n"
    "s_waitcnt vmcnt(10)\n"
    "buffer_load_dwordx4 v17, s[24:27], s46 offen offset:1024 lds\n"
    "s_waitcnt vmcnt(10)\n"
    "buffer_load_dwordx4 v18, s[24:27], s46 offen offset:2048 lds\n"
    "s_waitcnt vmcnt(10)\n"
    "buffer_load_dwordx4 v19, s[24:27], s46 offen offset:3072 lds\n"
    "s_add_u32 s46, s56, 65536\n"
    "s_add_u32 m0, s48, 114688\n"
    "s_waitcnt vmcnt(10)\n"
    "buffer_load_dwordx4 v16, s[24:27], s46 offen offset:0 lds\n"
    "s_waitcnt vmcnt(10)\n"
    "buffer_load_dwordx4 v17, s[24:27], s46 offen offset:1024 lds\n"
    "s_waitcnt vmcnt(10)\n"
    "buffer_load_dwordx4 v18, s[24:27], s46 offen offset:2048 lds\n"
    "s_waitcnt vmcnt(10)\n"
    "buffer_load_dwordx4 v19, s[24:27], s46 offen offset:3072 lds\n"
    "s_add_u32 s46, s57, 65536\n"
    "s_add_u32 m0, s48, 131072\n"
    "s_waitcnt vmcnt(16)\n"
    "s_barrier\n"
    "s_cmp_eq_u32 s49, 0\n"
    "s_cbranch_scc0 L_nfa\n"
    "s_mov_b64 exec, 15\n"
    "global_atomic_swap_x2 v24, v[2:3], s[6:7] sc1\n"
    "s_mov_b64 exec, -1\n"
    "L_nfa:\n"
    "s_waitcnt vmcnt(10)\n"
    "buffer_load_dwordx4 v16, s[24:27], s46 offen offset:0 lds\n"
    "s_waitcnt vmcnt(10)\n"
    "buffer_load_dwordx4 v17, s[24:27], s46 offen offset:1024 lds\n"
    "s_waitcnt vmcnt(10)\n"
    "buffer_load_dwordx4 v18, s[24:27], s46 offen offset:2048 lds\n"
    "s_waitcnt vmcnt(10)\n"
    "buffer_load_dwordx4 v19, s[24:27], s46 offen offset:3072 lds\n"
    "s_add_u32 s46, s59, 65536\n"
    "s_add_u32 m0, s48, 0\n"
    "s_waitcnt vmcnt(10)\n"
    "buffer_load_dwordx4 v16, s[24:27], s46 offen offset:0 lds\n"
    "s_waitcnt vmcnt(10)\n"
    "buffer_load_dwordx4 v17, s[24:27], s46 offen offset:1024 lds\n"
    "s_waitcnt vmcnt(14)\n"
    "s_barrier\n"
    "s_waitcnt vmcnt(7)\n"
    "buffer_load_dwordx4 v18, s[24:27], s46 offen offset:2048 lds\n"
    "s_waitcnt vmcnt(7)\n"
    "buffer_load_dwordx4 v19, s[24:27], s46 offen offset:3072 lds\n"
    "s_add_u32 s46, s60, 65536\n"
    "s_add_u32 m0, s48, 16384\n"
    "s_waitcnt vmcnt(7)\n"
    "buffer_load_dwordx4 v16, s[24:27], s46 offen offset:0 lds\n"
    "s_waitcnt vmcnt(7)\n"
    "buffer_load_dwordx4 v17, s[24:27], s46 offen offset:1024 lds\n"
    "s_waitcnt vmcnt(7)\n"
    "buffer_load_dwordx4 v18, s[24:27], s46 offen offset:2048 lds\n"
    "s_waitcnt vmcnt(7)\n"
    "buffer_load_dwordx4 v19, s[24:27], s46 offen offset:3072 lds\n"
    "s_add_u32 s46, s58, 65536\n"
    "s_add_u32 m0, s48, 147456\n"
    "s_waitcnt vmcnt(7)\n"
    "buffer_load_dwordx4 v16, s[24:27], s46 offen offset:0 lds\n"
    "s_cmp_eq_u32 s49, 0\n"
    "s_cbranch_scc0 L_npa\n"
    "s_mov_b64 exec, 0x3fff\n"
    "global_atomic_or_x2 v[252:253], v249, v[250:251], s[64:65] sc0 sc1\n"
    "s_mov_b64 exec, -1\n"
    "L_npa:\n"
    "s_waitcnt vmcnt(7)\n"
    "buffer_load_dwordx4 v17, s[24:27], s46 offen offset:1024 lds\n"
    "s_waitcnt vmcnt(7)\n"
    "buffer_load_dwordx4 v18, s[24:27], s46 offen offset:2048 lds\n"
    "s_cmp_eq_u32 s44, 3\n"
    "s_cbranch_scc0 L_nop3\n"
    "s_cmp_eq_u32 s49, 0\n"
    "s_cbranch_scc0 L_p3ok\n"
    "s_waitcnt vmcnt(2)\n"
    "s_mov_b64 exec, 0x3fff\n"
    "v_cmp_ne_u64 vcc, s[40:41], v[252:253]\n"
    "s_mov_b64 exec, -1\n"
    "s_cmp_eq_u64 vcc, 0\n"
    "s_cbranch_scc1 L_p3ok\n"
    "s_mov_b32 s63, 0x40000\n"
    "s_mov_b64 exec, 16383\n"
    "global_atomic_or_x2 v[252:253], v249, v[250:251], s[64:65] sc0 sc1\n"
    "L_p3:\n"
    "s_waitcnt vmcnt(0)\n"
    "v_cmp_ne_u64 vcc, s[40:41], v[252:253]\n"
    "s_nop 1\n"
    "s_cmp_eq_u64 vcc, 0\n"
    "s_cbranch_scc1 L_p3ok\n"
    "s_sleep 1\n"
    "global_atomic_or_x2 v[252:253], v249, v[250:251], s[64:65] sc0 sc1\n"
    "s_sub_u32 s63, s63, 1\n"
    "s_cmp_eq_u32 s63, 0\n"
    "s_cbranch_scc0 L_p3\n"
    "s_waitcnt vmcnt(0)\n"
    "L_p3ok:\n"
    "s_mov_b64 exec, -1\n"
    "s_barrier\n"
    "L_nop3:\n"
    "s_waitcnt vmcnt(7)\n"
    "buffer_load_dwordx4 v19, s[24:27], s46 offen offset:3072 lds\n"
    "s_add_u32 s46, s61, 65536\n"
    "s_add_u32 m0, s48, 32768\n"
    "s_waitcnt vmcnt(7)\n"
    "buffer_load_dwordx4 v16, s[24:27], s46 offen offset:0 lds\n"
    "s_waitcnt vmcnt(7)\n"
    "buffer_load_dwordx4 v17, s[24:27], s46 offen offset:1024 lds\n"
    "s_waitcnt vmcnt(7)\n"
    "buffer_load_dwordx4 v18, s[24:27], s46 offen offset:2048 lds\n"
    "s_waitcnt vmcnt(7)\n"
    "buffer_load_dwordx4 v19, s[24:27], s46 offen offset:3072 lds\n"
    "s_add_u32 s46, s62, 65536\n"
    "s_add_u32 m0, s48, 49152\n"
    "s_waitcnt vmcnt(7)\n"
    "buffer_load_dwordx4 v16, s[24:27], s46 offen offset:0 lds\n"
    "s_waitcnt vmcnt(7)\n"
    "buffer_load_dwordx4 v17, s[24:27], s46 offen offset:1024 lds\n"
    "s_waitcnt vmcnt(7)\n"
    "buffer_load_dwordx4 v18, s[24:27], s46 offen offset:2048 lds\n"
    "s_waitcnt vmcnt(7)\n"
    "buffer_load_dwordx4 v19, s[24:27], s46 offen offset:3072 lds\n"
    "s_cmp_eq_u32 s44, 3\n"
    "s_cbranch_scc1 L_nopa\n"
    "s_cmp_eq_u32 s49, 0\n"
    "s_cbranch_scc0 L_paok\n"
    "s_waitcnt vmcnt(11)\n"
    "s_mov_b64 exec, 0x3fff\n"
    "v_cmp_ne_u64 vcc, s[40:41], v[252:253]\n"
    "s_mov_b64 exec, -1\n"
    "s_cmp_eq_u64 vcc, 0\n"
    "s_cbranch_scc1 L_paok\n"
    "s_mov_b32 s63, 0x40000\n"
    "s_mov_b64 exec, 16383\n"
    "global_atomic_or_x2 v[252:253], v249, v[250:251], s[64:65] sc0 sc1\n"
    "L_pa:\n"
    "s_waitcnt vmcnt(0)\n"
    "v_cmp_ne_u64 vcc, s[40:41], v[252:253]\n"
    "s_nop 1\n"
    "s_cmp_eq_u64 vcc, 0\n"
    "s_cbranch_scc1 L_paok\n"
    "s_sleep 1\n"
    "global_atomic_or_x2 v[252:253], v249, v[250:251], s[64:65] sc0 sc1\n"
    "s_sub_u32 s63, s63, 1\n"
    "s_cmp_eq_u32 s63, 0\n"
    "s_cbranch_scc0 L_pa\n"
    "s_waitcnt vmcnt(0)\n"
    "L_paok:\n"
    "s_mov_b64 exec, -1\n"
    "s_barrier\n"
    "L_nopa:\n"
    "s_barrier\n"
    "s_cmp_eq_u32 s49, 0\n"
    "s_cbranch_scc0 L_nfb\n"
    "s_mov_b32 exec_lo, s97\n"
    "s_mov_b32 exec_hi, 0\n"
    "global_atomic_swap_x2 v24, v[2:3], s[6:7] offset:2048 sc1\n"
    "s_mov_b64 exec, -1\n"
    "L_nfb:\n"
    "s_waitcnt vmcnt(0) lgkmcnt(0)\n"
    "L_hlp:\n"
    "s_cmp_eq_u32 s49, 0\n"
    "s_cbranch_scc0 L_hpollok\n"
    "s_lshr_b32 s67, s43, 1\n"
    "s_lshl_b32 s67, s67, 12\n"
    "s_and_b32 s68, s43, 1\n"
    "s_lshl_b32 s68, s68, 3\n"
    "s_add_u32 s68, s68, s44\n"
    "s_and_b32 s68, s68, 3\n"
    "s_lshl_b32 s68, s68, 8\n"
    "s_add_u32 s67, s67, s68\n"
    "s_add_u32 s67, s67, 7342080\n"
    "s_add_u32 s64, s6, s67\n"
    "s_addc_u32 s65, s7, 0\n"
    "s_mov_b32 s63, 0x40000\n"
    "s_mov_b64 exec, 16383\n"
    "global_atomic_or_x2 v[252:253], v249, v[250:251], s[64:65] sc0 sc1\n"
    "L_hpoll:\n"
    "s_waitcnt vmcnt(0)\n"
    "v_cmp_ne_u64 vcc, s[40:41], v[252:253]\n"
    "s_nop 1\n"
    "s_cmp_eq_u64 vcc, 0\n"
    "s_cbranch_scc1 L_hpollok\n"
    "s_sleep 1\n"
    "global_atomic_or_x2 v[252:253], v249, v[250:251], s[64:65] sc0 sc1\n"
    "s_sub_u32 s63, s63, 1\n"
    "s_cmp_eq_u32 s63, 0\n"
    "s_cbranch_scc0 L_hpoll\n"
    "s_waitcnt vmcnt(0)\n"
    "L_hpollok:\n"
    "s_mov_b64 exec, -1\n"
    "s_barrier\n"
    "s_cmp_eq_u32 %2, 231\n"
    "s_cselect_b32 s67, s36, 0\n"
    "s_cmp_eq_u32 s67, 4\n"
    "s_cbranch_scc1 L_own\n"
    "s_endpgm\n"
    "L_lidle:\n"
    "s_lshr_b32 s43, s37, 1\n"
    "s_lshl_b32 s43, s43, 3\n"
    "s_add_u32 s43, s43, 7\n"
    "s_and_b32 s44, s37, 1\n"
    "s_lshl_b32 s44, s44, 2\n"
    "s_add_u32 s44, s44, s51\n"
    "s_sub_u32 s49, s36, 4\n"
    "v_lshrrev_b32 v2, 3, v1\n"
    "v_and_b32 v3, 7, v1\n"
    "s_lshl_b32 s48, s49, 12\n"
    "s_lshl_b32 s50, s39, 17\n"
    "v_mov_b32 v7, 0\n"
    "s_mov_b32 s18, 131072\n"
    "s_mov_b32 s19, 0x20000\n"
    "s_and_b32 s45, s45, 3\n"
    "s_add_u32 s68, s45, 5\n"
    "s_and_b32 s68, s68, 7\n"
    "s_lshl_b32 s68, s68, 7\n"
    "s_add_u32 s68, s68, 16384\n"
    "s_cmp_eq_u32 s45, 3\n"
    "s_cselect_b32 s67, 4, s45\n"
    "s_add_u32 s67, s67, 1\n"
    "s_lshl_b32 s67, s67, 7\n"
    "s_add_u32 s67, s67, 16384\n"
    "v_and_b32 v6, 15, v1\n"
    "v_lshlrev_b32 v6, 10, v6\n"
    "v_add_u32 v50, s68, v6\n"
    "s_mov_b64 exec, 0xffff\n"
    "v_add_u32 v50, s67, v6\n"
    "s_mov_b32 exec_lo, 0\n"
    "s_mov_b32 exec_hi, -1\n"
    "v_and_b32 v6, 31, v1\n"
    "v_lshlrev_b32 v6, 10, v6\n"
    "v_add_u32 v50, 384, v6\n"
    "s_mov_b64 exec, -1\n"
    "s_lshl_b32 s96, s49, 15\n"
    "s_lshl_b32 s67, s37, 2\n"
    "s_add_u32 s67, s67, s38\n"
    "s_sub_u32 s67, s67, 28\n"
    "s_mul_i32 s97, s67, 57344\n"
    "v_min_u32 v51, 55, v1\n"
    "v_lshlrev_b32 v51, 10, v51\n"
    "s_sub_u32 s67, s38, 28\n"
    "s_lshl_b32 s67, s67, 2\n"
    "s_add_u32 s67, s67, s49\n"
    "s_mul_i32 s67, s67, 114688\n"
    "s_and_b32 s68, s37, 1\n"
    "s_mul_i32 s68, s68, 1792\n"
    "s_add_u32 s67, s67, s68\n"
    "s_add_u32 s47, s67, 128\n"
    "v_and_b32 v5, 31, v1\n"
    "v_mul_u32_u24 v5, 3584, v5\n"
    "v_lshrrev_b32 v6, 5, v1\n"
    "s_lshr_b32 s67, s37, 1\n"
    "s_lshl_b32 s46, s67, 1\n"
    "v_add_u32 v14, s46, v6\n"
    "v_min_u32 v14, 6, v14\n"
    "v_lshl_add_u32 v52, v14, 8, v5\n"
    "s_waitcnt lgkmcnt(0)\n"
    "s_add_u32 s16, s8, s50\n"
    "s_addc_u32 s17, s9, 0\n"
    "s_bitcmp1_b32 s37, 0\n"
    "s_cselect_b32 exec_hi, -1, 0\n"
    "buffer_load_dword v58, v50, s[16:19], s96 offen\n"
    "s_mov_b64 exec, -1\n"
    "s_cmp_eq_u32 s49, 0\n"
    "s_cbranch_scc0 L_noc3\n"
    "s_sub_u32 s68, 384, s14\n"
    "s_and_b32 s68, s68, 0x3fc\n"
    "s_add_u32 s67, s97, s68\n"
    "s_add_u32 s68, s14, s67\n"
    "s_addc_u32 s69, s15, 0\n"
    "global_load_dword v59, v51, s[68:69]\n"
    "L_noc3:\n"
    "s_add_u32 s68, s10, s47\n"
    "s_addc_u32 s69, s11, 0\n"
    "global_load_dword v60, v52, s[68:69]\n"
    "s_add_u32 s64, s6, 7409664\n"
    "s_addc_u32 s65, s7, 0\n"
    "global_load_dwordx2 v[20:21], v7, s[64:65] sc1\n"
    "s_branch L_lidle2\n"
    "L_cpro:\n"
    "s_lshr_b32 s67, s38, 1\n"
    "s_lshr_b32 s69, s38, 2\n"
    "s_xor_b32 s69, s67, s69\n"
    "s_andn2_b32 s69, s38, s69\n"
    "s_and_b32 s69, s69, 1\n"
    "s_cmp_lt_u32 s38, 24\n"
    "s_cselect_b32 s69, s69, 0\n"
    "s_mul_i32 s69, s69, 6\n"
    "s_xor_b32 s69, s69, s38\n"
    "s_lshr_b32 s67, s69, 2\n"
    "s_and_b32 s69, s69, 3\n"
    "s_lshr_b32 s43, s37, 1\n"
    "s_lshl_b32 s43, s43, 3\n"
    "s_add_u32 s43, s43, s67\n"
    "s_and_b32 s44, s37, 1\n"
    "s_lshl_b32 s44, s44, 2\n"
    "s_add_u32 s44, s44, s69\n"
    "v_lshrrev_b32 v5, 4, v1\n"
    "v_lshlrev_b32 v6, 3, v5\n"
    "v_mov_b32 v7, 0\n"
    "s_lshl_b32 s51, s42, 8\n"
    "s_cmp_ge_u32 s38, 28\n"
    "s_cbranch_scc1 L_cidle\n"
    "s_waitcnt lgkmcnt(0)\n"
    "v_add_u32 v7, 163584, v6\n"
    "v_and_b32 v2, 15, v1\n"
    "v_lshrrev_b32 v3, 1, v2\n"
    "v_xor_b32 v3, v3, v5\n"
    "v_lshlrev_b32 v3, 4, v3\n"
    "v_lshl_or_b32 v3, v2, 7, v3\n"
    "s_lshl_b32 s51, s36, 12\n"
    "v_add_u32 v20, s51, v3\n"
    "v_xor_b32 v21, 64, v20\n"
    "v_add_u32 v22, 0x10000, v20\n"
    "v_add_u32 v23, 0x10000, v21\n"
    "v_bfe_u32 v3, v1, 2, 2\n"
    "v_lshl_add_u32 v6, v5, 3, v3\n"
    "v_lshrrev_b32 v3, 1, v3\n"
    "v_and_b32 v2, 1, v5\n"
    "v_lshl_or_b32 v3, v2, 1, v3\n"
    "v_lshlrev_b32 v3, 5, v3\n"
    "v_lshl_or_b32 v3, v6, 7, v3\n"
    "v_and_b32 v2, 3, v1\n"
    "v_lshl_or_b32 v8, v2, 3, v3\n"
    "v_xor_b32 v9, 32, v8\n"
    "v_xor_b32 v10, 64, v8\n"
    "v_xor_b32 v11, 96, v8\n"
    "v_add_u32 v12, 65536, v8\n"
    "v_add_u32 v13, 65536, v9\n"
    "v_add_u32 v14, 65536, v10\n"
    "v_add_u32 v15, 65536, v11\n"
    "v_add_u32 v16, 131072, v8\n"
    "v_add_u32 v17, 131072, v9\n"
    "v_add_u32 v18, 131072, v10\n"
    "v_add_u32 v19, 131072, v11\n"
    "s_lshl_b32 s51, s36, 12\n"
    "v_lshlrev_b32 v6, 9, v5\n"
    "v_sub_u32 v6, s51, v6\n"
    "s_lshr_b32 s49, s36, 1\n"
    "s_and_b32 s50, s36, 1\n"
    "s_lshl_b32 s51, s39, 1\n"
    "s_add_u32 s51, s51, s49\n"
    "s_mul_i32 s68, s51, 224\n"
    "s_mul_i32 s69, s50, 112\n"
    "s_add_u32 s68, s68, s69\n"
    "s_lshl_b32 s69, s42, 2\n"
    "s_add_u32 s68, s68, s69\n"
    "s_lshl_b32 s68, s68, 10\n"
    "s_add_u32 s28, s6, s68\n"
    "s_addc_u32 s29, s7, 0\n"
    "s_mov_b32 s30, 114688\n"
    "s_mov_b32 s31, 0x20000\n"
    "s_mov_b32 s52, 0xbfb8aa3b\n"
    "s_mov_b32 s53, 0xbfb8aa3b\n"
    "s_mov_b32 s54, 0.5\n"
    "s_mov_b32 s55, 0.5\n"
    "s_mov_b32 s46, 57344\n"
    "s_lshl_b32 s56, s66, 11\n"
    "s_add_u32 s66, s66, 1\n"
    "s_cmp_eq_u32 s66, 7\n"
    "s_cselect_b32 s66, 0, s66\n"
    "s_lshl_b32 s57, s66, 11\n"
    "s_add_u32 s66, s66, 1\n"
    "s_cmp_eq_u32 s66, 7\n"
    "s_cselect_b32 s66, 0, s66\n"
    "s_lshl_b32 s58, s66, 11\n"
    "s_add_u32 s66, s66, 1\n"
    "s_cmp_eq_u32 s66, 7\n"
    "s_cselect_b32 s66, 0, s66\n"
    "s_lshl_b32 s59, s66, 11\n"
    "s_add_u32 s66, s66, 1\n"
    "s_cmp_eq_u32 s66, 7\n"
    "s_cselect_b32 s66, 0, s66\n"
    "s_lshl_b32 s60, s66, 11\n"
    "s_add_u32 s66, s66, 1\n"
    "s_cmp_eq_u32 s66, 7\n"
    "s_cselect_b32 s66, 0, s66\n"
    "s_lshl_b32 s61, s66, 11\n"
    "s_add_u32 s66, s66, 1\n"
    "s_cmp_eq_u32 s66, 7\n"
    "s_cselect_b32 s66, 0, s66\n"
    "s_lshl_b32 s62, s66, 11\n"
    "s_add_u32 s67, s36, 1\n"
    "s_and_b32 s67, s67, 3\n"
    "s_mul_i32 s16, s67, 57344\n"
    "s_add_u32 s67, s36, 3\n"
    "s_and_b32 s67, s67, 3\n"
    "s_lshl_b32 s67, s67, 12\n"
    "s_add_u32 s8, s67, 65536\n"
    "s_add_u32 s67, s36, 2\n"
    "s_and_b32 s67, s67, 3\n"
    "s_mul_i32 s17, s67, 57344\n"
    "s_add_u32 s67, s36, 2\n"
    "s_and_b32 s67, s67, 3\n"
    "s_lshl_b32 s67, s67, 12\n"
    "s_add_u32 s9, s67, 81920\n"
    "s_add_u32 s67, s36, 3\n"
    "s_and_b32 s67, s67, 3\n"
    "s_mul_i32 s18, s67, 57344\n"
    "s_add_u32 s67, s36, 1\n"
    "s_and_b32 s67, s67, 3\n"
    "s_lshl_b32 s67, s67, 12\n"
    "s_add_u32 s10, s67, 98304\n"
    "s_add_u32 s67, s36, 4\n"
    "s_and_b32 s67, s67, 3\n"
    "s_mul_i32 s19, s67, 57344\n"
    "s_add_u32 s67, s36, 0\n"
    "s_and_b32 s67, s67, 3\n"
    "s_lshl_b32 s67, s67, 12\n"
    "s_add_u32 s11, s67, 65536\n"
    "s_mul_i32 s51, s43, 224\n"
    "s_mul_i32 s68, s36, 14\n"
    "s_add_u32 s51, s51, s68\n"
    "s_lshl_b32 s51, s51, 10\n"
    "s_add_u32 s20, s6, s51\n"
    "s_addc_u32 s21, s7, 0\n"
    "s_mov_b32 s22, 186368\n"
    "s_mov_b32 s23, 0x20000\n"
    "s_lshl_b32 s67, s43, 2\n"
    "s_add_u32 s67, s67, s36\n"
    "s_lshl_b32 s67, s67, 14\n"
    "s_lshl_b32 s68, s44, 7\n"
    "s_add_u32 s67, s67, s68\n"
    "s_add_u32 s92, s4, s67\n"
    "s_addc_u32 s93, s5, 0\n"
    "s_sub_u32 s94, 2097152, s67\n"
    "s_mov_b32 s95, 0x20000\n"
    "s_mov_b32 s12, 8192\n"
    "s_barrier\n"
    "ds_read_b64 v[56:57], v7 offset:0\n"
    "ds_read_b64 v[58:59], v7 offset:32\n"
    "ds_read_b64 v[60:61], v7 offset:64\n"
    "ds_read_b64 v[62:63], v7 offset:96\n"
    "ds_read_b64 v[102:103], v7 offset:128\n"
    "ds_read_b64 v[110:111], v7 offset:160\n"
    "ds_read_b64 v[118:119], v7 offset:192\n"
    "ds_read_b64 v[126:127], v7 offset:224\n"
    "s_waitcnt vmcnt(0)\n"
    "ds_read_b64_tr_b16 v[24:25], v8 offset:16384\n"
    "ds_read_b64_tr_b16 v[26:27], v8 offset:16896\n"
    "ds_read_b128 v[128:131], v20 offset:0\n"
    "ds_read_b128 v[132:135], v20 offset:2048\n"
    "ds_read_b64_tr_b16 v[28:29], v9 offset:16384\n"
    "ds_read_b64_tr_b16 v[30:31], v9 offset:16896\n"
    "ds_read_b64_tr_b16 v[32:33], v10 offset:16384\n"
    "ds_read_b64_tr_b16 v[34:35], v10 offset:16896\n"
    "ds_read_b64_tr_b16 v[36:37], v11 offset:16384\n"
    "ds_read_b64_tr_b16 v[38:39], v11 offset:16896\n"
    "s_waitcnt lgkmcnt(10)\n"
    "v_lshlrev_b32 v64, 16, v56\n"
    "v_and_b32 v65, 0xffff0000, v56\n"
    "v_lshlrev_b32 v66, 16, v57\n"
    "v_and_b32 v67, 0xffff0000, v57\n"
    "v_mov_b32 v68, v64\n"
    "v_mov_b32 v69, v65\n"
    "v_mov_b32 v70, v66\n"
    "v_mov_b32 v71, v67\n"
    "v_lshlrev_b32 v72, 16, v58\n"
    "v_and_b32 v73, 0xffff0000, v58\n"
    "v_lshlrev_b32 v74, 16, v59\n"
    "v_and_b32 v75, 0xffff0000, v59\n"
    "v_mov_b32 v76, v72\n"
    "v_mov_b32 v77, v73\n"
    "v_mov_b32 v78, v74\n"
    "v_mov_b32 v79, v75\n"
    "v_lshlrev_b32 v80, 16, v60\n"
    "v_and_b32 v81, 0xffff0000, v60\n"
    "v_lshlrev_b32 v82, 16, v61\n"
    "v_and_b32 v83, 0xffff0000, v61\n"
    "v_mov_b32 v84, v80\n"
    "v_mov_b32 v85, v81\n"
    "v_mov_b32 v86, v82\n"
    "v_mov_b32 v87, v83\n"
    "v_lshlrev_b32 v88, 16, v62\n"
    "v_and_b32 v89, 0xffff0000, v62\n"
    "v_lshlrev_b32 v90, 16, v63\n"
    "v_and_b32 v91, 0xffff0000, v63\n"
    "v_mov_b32 v92, v88\n"
    "v_mov_b32 v93, v89\n"
    "v_mov_b32 v94, v90\n"
    "v_mov_b32 v95, v91\n"
    "s_waitcnt lgkmcnt(0)\n"
    "v_mfma_f32_16x16x32_bf16 v[64:67], v[24:27], v[128:131], v[64:67]\n"
    "ds_read_b64_tr_b16 v[40:41], v8 offset:20480\n"
    "ds_read_b64_tr_b16 v[42:43], v8 offset:20992\n"
    "v_mfma_f32_16x16x32_bf16 v[68:71], v[24:27], v[132:135], v[68:71]\n"
    "ds_read_b128 v[136:139], v21 offset:0\n"
    "ds_read_b128 v[140:143], v21 offset:2048\n"
    "v_mfma_f32_16x16x32_bf16 v[76:79], v[28:31], v[132:135], v[76:79]\n"
    "ds_read_b64_tr_b16 v[44:45], v9 offset:20480\n"
    "ds_read_b64_tr_b16 v[46:47], v9 offset:20992\n"
    "v_mfma_f32_16x16x32_bf16 v[72:75], v[28:31], v[128:131], v[72:75]\n"
    "ds_read_b64_tr_b16 v[48:49], v10 offset:20480\n"
    "ds_read_b64_tr_b16 v[50:51], v10 offset:20992\n"
    "v_mfma_f32_16x16x32_bf16 v[80:83], v[32:35], v[128:131], v[80:83]\n"
    "ds_read_b64_tr_b16 v[52:53], v11 offset:20480\n"
    "ds_read_b64_tr_b16 v[54:55], v11 offset:20992\n"
    "v_mfma_f32_16x16x32_bf16 v[84:87], v[32:35], v[132:135], v[84:87]\n"
    "v_lshlrev_b32 v96, 16, v102\n"
    "v_and_b32 v97, 0xffff0000, v102\n"
    "v_mfma_f32_16x16x32_bf16 v[92:95], v[36:39], v[132:135], v[92:95]\n"
    "v_lshlrev_b32 v98, 16, v103\n"
    "v_and_b32 v99, 0xffff0000, v103\n"
    "v_mfma_f32_16x16x32_bf16 v[88:91], v[36:39], v[128:131], v[88:91]\n"
    "v_mov_b32 v100, v96\n"
    "v_mov_b32 v101, v97\n"
    "s_mov_b64 exec, 0\n"
    "s_branch L_pad0\n"
    "L_back0:\n"
    "s_mov_b64 exec, -1\n"
    "s_waitcnt lgkmcnt(0)\n"
    "s_barrier\n"
    "v_mfma_f32_16x16x32_bf16 v[64:67], v[40:43], v[136:139], v[64:67]\n"
    "ds_read_b64_tr_b16 v[24:25], v8 offset:40960\n"
    "ds_read_b64_tr_b16 v[26:27], v8 offset:41472\n"
    "v_mfma_f32_16x16x32_bf16 v[68:71], v[40:43], v[140:143], v[68:71]\n"
    "ds_read_b128 v[144:147], v20 offset:24576\n"
    "ds_read_b128 v[148:151], v20 offset:26624\n"
    "v_mfma_f32_16x16x32_bf16 v[76:79], v[44:47], v[140:143], v[76:79]\n"
    "ds_read_b64_tr_b16 v[28:29], v9 offset:40960\n"
    "ds_read_b64_tr_b16 v[30:31], v9 offset:41472\n"
    "v_mfma_f32_16x16x32_bf16 v[72:75], v[44:47], v[136:139], v[72:75]\n"
    "ds_read_b64_tr_b16 v[32:33], v10 offset:40960\n"
    "ds_read_b64_tr_b16 v[34:35], v10 offset:41472\n"
    "v_mfma_f32_16x16x32_bf16 v[80:83], v[48:51], v[136:139], v[80:83]\n"
    "ds_read_b64_tr_b16 v[36:37], v11 offset:40960\n"
    "ds_read_b64_tr_b16 v[38:39], v11 offset:41472\n"
    "v_mfma_f32_16x16x32_bf16 v[84:87], v[48:51], v[140:143], v[84:87]\n"
    "v_mov_b32 v102, v98\n"
    "v_mov_b32 v103, v99\n"
    "v_mfma_f32_16x16x32_bf16 v[92:95], v[52:55], v[140:143], v[92:95]\n"
    "v_lshlrev_b32 v104, 16, v110\n"
    "v_and_b32 v105, 0xffff0000, v110\n"
    "v_mfma_f32_16x16x32_bf16 v[88:91], v[52:55], v[136:139], v[88:91]\n"
    "v_lshlrev_b32 v106, 16, v111\n"
    "v_and_b32 v107, 0xffff0000, v111\n"
    "s_waitcnt lgkmcnt(0)\n"
    "v_mfma_f32_16x16x32_bf16 v[64:67], v[24:27], v[144:147], v[64:67]\n"
    "ds_read_b64_tr_b16 v[40:41], v8 offset:45056\n"
    "ds_read_b64_tr_b16 v[42:43], v8 offset:45568\n"
    "v_mfma_f32_16x16x32_bf16 v[68:71], v[24:27], v[148:151], v[68:71]\n"
    "ds_read_b128 v[152:155], v21 offset:24576\n"
    "ds_read_b128 v[156:159], v21 offset:26624\n"
    "v_mfma_f32_16x16x32_bf16 v[76:79], v[28:31], v[148:151], v[76:79]\n"
    "ds_read_b64_tr_b16 v[44:45], v9 offset:45056\n"
    "ds_read_b64_tr_b16 v[46:47], v9 offset:45568\n"
    "v_mfma_f32_16x16x32_bf16 v[72:75], v[28:31], v[144:147], v[72:75]\n"
    "ds_read_b64_tr_b16 v[48:49], v10 offset:45056\n"
    "ds_read_b64_tr_b16 v[50:51], v10 offset:45568\n"
    "v_mfma_f32_16x16x32_bf16 v[80:83], v[32:35], v[144:147], v[80:83]\n"
    "ds_read_b64_tr_b16 v[52:53], v11 offset:45056\n"
    "ds_read_b64_tr_b16 v[54:55], v11 offset:45568\n"
    "v_mfma_f32_16x16x32_bf16 v[84:87], v[32:35], v[148:151], v[84:87]\n"
    "v_mov_b32 v108, v104\n"
    "v_mov_b32 v109, v105\n"
    "v_mfma_f32_16x16x32_bf16 v[92:95], v[36:39], v[148:151], v[92:95]\n"
    "v_mov_b32 v110, v106\n"
    "v_mov_b32 v111, v107\n"
    "v_mfma_f32_16x16x32_bf16 v[88:91], v[36:39], v[144:147], v[88:91]\n"
    "v_lshlrev_b32 v112, 16, v118\n"
    "v_and_b32 v113, 0xffff0000, v118\n"
    "s_mov_b64 exec, 0\n"
    "s_branch L_pad1\n"
    "L_back1:\n"
    "s_mov_b64 exec, -1\n"
    "s_waitcnt lgkmcnt(0)\n"
    "s_barrier\n"
    "v_mfma_f32_16x16x32_bf16 v[64:67], v[40:43], v[152:155], v[64:67]\n"
    "ds_read_b64_tr_b16 v[24:25], v12 offset:0\n"
    "ds_read_b64_tr_b16 v[26:27], v12 offset:512\n"
    "v_mfma_f32_16x16x32_bf16 v[68:71], v[40:43], v[156:159], v[68:71]\n"
    "ds_read_b128 v[160:163], v20 offset:49152\n"
    "ds_read_b128 v[164:167], v20 offset:51200\n"
    "v_mfma_f32_16x16x32_bf16 v[76:79], v[44:47], v[156:159], v[76:79]\n"
    "ds_read_b64_tr_b16 v[28:29], v13 offset:0\n"
    "ds_read_b64_tr_b16 v[30:31], v13 offset:512\n"
    "v_mfma_f32_16x16x32_bf16 v[72:75], v[44:47], v[152:155], v[72:75]\n"
    "ds_read_b64_tr_b16 v[32:33], v14 offset:0\n"
    "ds_read_b64_tr_b16 v[34:35], v14 offset:512\n"
    "v_mfma_f32_16x16x32_bf16 v[80:83], v[48:51], v[152:155], v[80:83]\n"
    "ds_read_b64_tr_b16 v[36:37], v15 offset:0\n"
    "ds_read_b64_tr_b16 v[38:39], v15 offset:512\n"
    "v_mfma_f32_16x16x32_bf16 v[84:87], v[48:51], v[156:159], v[84:87]\n"
    "v_lshlrev_b32 v114, 16, v119\n"
    "v_and_b32 v115, 0xffff0000, v119\n"
    "v_mfma_f32_16x16x32_bf16 v[92:95], v[52:55], v[156:159], v[92:95]\n"
    "v_mov_b32 v116, v112\n"
    "v_mov_b32 v117, v113\n"
    "v_mfma_f32_16x16x32_bf16 v[88:91], v[52:55], v[152:155], v[88:91]\n"
    "v_mov_b32 v118, v114\n"
    "v_mov_b32 v119, v115\n"
    "s_waitcnt lgkmcnt(0)\n"
    "v_mfma_f32_16x16x32_bf16 v[64:67], v[24:27], v[160:163], v[64:67]\n"
    "ds_read_b64_tr_b16 v[40:41], v12 offset:4096\n"
    "ds_read_b64_tr_b16 v[42:43], v12 offset:4608\n"
    "v_mfma_f32_16x16x32_bf16 v[68:71], v[24:27], v[164:167], v[68:71]\n"
    "ds_read_b128 v[168:171], v21 offset:49152\n"
    "ds_read_b128 v[172:175], v21 offset:51200\n"
    "v_mfma_f32_16x16x32_bf16 v[76:79], v[28:31], v[164:167], v[76:79]\n"
    "ds_read_b64_tr_b16 v[44:45], v13 offset:4096\n"
    "ds_read_b64_tr_b16 v[46:47], v13 offset:4608\n"
    "v_mfma_f32_16x16x32_bf16 v[72:75], v[28:31], v[160:163], v[72:75]\n"
    "ds_read_b64_tr_b16 v[48:49], v14 offset:4096\n"
    "ds_read_b64_tr_b16 v[50:51], v14 offset:4608\n"
    "v_mfma_f32_16x16x32_bf16 v[80:83], v[32:35], v[160:163], v[80:83]\n"
    "ds_read_b64_tr_b16 v[52:53], v15 offset:4096\n"
    "ds_read_b64_tr_b16 v[54:55], v15 offset:4608\n"
    "v_mfma_f32_16x16x32_bf16 v[84:87], v[32:35], v[164:167], v[84:87]\n"
    "v_lshlrev_b32 v120, 16, v126\n"
    "v_and_b32 v121, 0xffff0000, v126\n"
    "v_mfma_f32_16x16x32_bf16 v[92:95], v[36:39], v[164:167], v[92:95]\n"
    "v_lshlrev_b32 v122, 16, v127\n"
    "v_and_b32 v123, 0xffff0000, v127\n"
    "v_mfma_f32_16x16x32_bf16 v[88:91], v[36:39], v[160:163], v[88:91]\n"
    "v_mov_b32 v124, v120\n"
    "v_mov_b32 v125, v121\n"
    "s_waitcnt lgkmcnt(0)\n"
    "s_barrier\n"
    "v_mfma_f32_16x16x32_bf16 v[64:67], v[40:43], v[168:171], v[64:67]\n"
    "ds_read_b64_tr_b16 v[24:25], v12 offset:24576\n"
    "ds_read_b64_tr_b16 v[26:27], v12 offset:25088\n"
    "v_mfma_f32_16x16x32_bf16 v[68:71], v[40:43], v[172:175], v[68:71]\n"
    "ds_read_b128 v[176:179], v22 offset:8192\n"
    "ds_read_b128 v[180:183], v22 offset:10240\n"
    "v_mfma_f32_16x16x32_bf16 v[76:79], v[44:47], v[172:175], v[76:79]\n"
    "ds_read_b64_tr_b16 v[28:29], v13 offset:24576\n"
    "ds_read_b64_tr_b16 v[30:31], v13 offset:25088\n"
    "v_mfma_f32_16x16x32_bf16 v[72:75], v[44:47], v[168:171], v[72:75]\n"
    "ds_read_b64_tr_b16 v[32:33], v14 offset:24576\n"
    "ds_read_b64_tr_b16 v[34:35], v14 offset:25088\n"
    "v_mfma_f32_16x16x32_bf16 v[80:83], v[48:51], v[168:171], v[80:83]\n"
    "ds_read_b64_tr_b16 v[36:37], v15 offset:24576\n"
    "ds_read_b64_tr_b16 v[38:39], v15 offset:25088\n"
    "v_mfma_f32_16x16x32_bf16 v[84:87], v[48:51], v[172:175], v[84:87]\n"
    "v_mov_b32 v126, v122\n"
    "v_mov_b32 v127, v123\n"
    "v_mfma_f32_16x16x32_bf16 v[92:95], v[52:55], v[172:175], v[92:95]\n"
    "v_mfma_f32_16x16x32_bf16 v[88:91], v[52:55], v[168:171], v[88:91]\n"
    "s_waitcnt lgkmcnt(0)\n"
    "v_mfma_f32_16x16x32_bf16 v[64:67], v[24:27], v[176:179], v[64:67]\n"
    "ds_read_b64_tr_b16 v[40:41], v12 offset:28672\n"
    "ds_read_b64_tr_b16 v[42:43], v12 offset:29184\n"
    "v_mfma_f32_16x16x32_bf16 v[68:71], v[24:27], v[180:183], v[68:71]\n"
    "ds_read_b128 v[184:187], v23 offset:8192\n"
    "ds_read_b128 v[188:191], v23 offset:10240\n"
    "v_mfma_f32_16x16x32_bf16 v[76:79], v[28:31], v[180:183], v[76:79]\n"
    "ds_read_b64_tr_b16 v[44:45], v13 offset:28672\n"
    "ds_read_b64_tr_b16 v[46:47], v13 offset:29184\n"
    "v_mfma_f32_16x16x32_bf16 v[72:75], v[28:31], v[176:179], v[72:75]\n"
    "ds_read_b64_tr_b16 v[48:49], v14 offset:28672\n"
    "ds_read_b64_tr_b16 v[50:51], v14 offset:29184\n"
    "v_mfma_f32_16x16x32_bf16 v[80:83], v[32:35], v[176:179], v[80:83]\n"
    "ds_read_b64_tr_b16 v[52:53], v15 offset:28672\n"
    "ds_read_b64_tr_b16 v[54:55], v15 offset:29184\n"
    "v_mfma_f32_16x16x32_bf16 v[84:87], v[32:35], v[180:183], v[84:87]\n"
    "v_mfma_f32_16x16x32_bf16 v[92:95], v[36:39], v[180:183], v[92:95]\n"
    "v_mfma_f32_16x16x32_bf16 v[88:91], v[36:39], v[176:179], v[88:91]\n"
    "s_waitcnt lgkmcnt(0)\n"
    "s_barrier\n"
    "v_mfma_f32_16x16x32_bf16 v[64:67], v[40:43], v[184:187], v[64:67]\n"
    "ds_read_b64_tr_b16 v[24:25], v8 offset:16384\n"
    "ds_read_b64_tr_b16 v[26:27], v8 offset:16896\n"
    "v_mfma_f32_16x16x32_bf16 v[68:71], v[40:43], v[188:191], v[68:71]\n"
    "ds_read_b128 v[192:195], v20 offset:0\n"
    "ds_read_b128 v[196:199], v20 offset:2048\n"
    "v_mfma_f32_16x16x32_bf16 v[76:79], v[44:47], v[188:191], v[76:79]\n"
    "ds_read_b64_tr_b16 v[28:29], v9 offset:16384\n"
    "ds_read_b64_tr_b16 v[30:31], v9 offset:16896\n"
    "v_mfma_f32_16x16x32_bf16 v[72:75], v[44:47], v[184:187], v[72:75]\n"
    "ds_read_b64_tr_b16 v[32:33], v10 offset:16384\n"
    "ds_read_b64_tr_b16 v[34:35], v10 offset:16896\n"
    "v_mfma_f32_16x16x32_bf16 v[80:83], v[48:51], v[184:187], v[80:83]\n"
    "ds_read_b64_tr_b16 v[36:37], v11 offset:16384\n"
    "ds_read_b64_tr_b16 v[38:39], v11 offset:16896\n"
    "v_mfma_f32_16x16x32_bf16 v[84:87], v[48:51], v[188:191], v[84:87]\n"
    "v_mfma_f32_16x16x32_bf16 v[92:95], v[52:55], v[188:191], v[92:95]\n"
    "v_mfma_f32_16x16x32_bf16 v[88:91], v[52:55], v[184:187], v[88:91]\n"
    "s_waitcnt lgkmcnt(0)\n"
    "v_mfma_f32_16x16x32_bf16 v[64:67], v[24:27], v[192:195], v[64:67]\n"
    "ds_read_b64_tr_b16 v[40:41], v8 offset:20480\n"
    "ds_read_b64_tr_b16 v[42:43], v8 offset:20992\n"
    "v_mfma_f32_16x16x32_bf16 v[68:71], v[24:27], v[196:199], v[68:71]\n"
    "ds_read_b128 v[200:203], v21 offset:0\n"
    "ds_read_b128 v[204:207], v21 offset:2048\n"
    "v_mfma_f32_16x16x32_bf16 v[76:79], v[28:31], v[196:199], v[76:79]\n"
    "ds_read_b64_tr_b16 v[44:45], v9 offset:20480\n"
    "ds_read_b64_tr_b16 v[46:47], v9 offset:20992\n"
    "v_mfma_f32_16x16x32_bf16 v[72:75], v[28:31], v[192:195], v[72:75]\n"
    "ds_read_b64_tr_b16 v[48:49], v10 offset:20480\n"
    "ds_read_b64_tr_b16 v[50:51], v10 offset:20992\n"
    "v_mfma_f32_16x16x32_bf16 v[80:83], v[32:35], v[192:195], v[80:83]\n"
    "ds_read_b64_tr_b16 v[52:53], v11 offset:20480\n"
    "ds_read_b64_tr_b16 v[54:55], v11 offset:20992\n"
    "v_mfma_f32_16x16x32_bf16 v[84:87], v[32:35], v[196:199], v[84:87]\n"
    "v_mfma_f32_16x16x32_bf16 v[92:95], v[36:39], v[196:199], v[92:95]\n"
    "v_mfma_f32_16x16x32_bf16 v[88:91], v[36:39], v[192:195], v[88:91]\n"
    "s_waitcnt lgkmcnt(0)\n"
    "s_barrier\n"
    "v_mfma_f32_16x16x32_bf16 v[64:67], v[40:43], v[200:203], v[64:67]\n"
    "ds_read_b64_tr_b16 v[24:25], v8 offset:40960\n"
    "ds_read_b64_tr_b16 v[26:27], v8 offset:41472\n"
    "v_mfma_f32_16x16x32_bf16 v[68:71], v[40:43], v[204:207], v[68:71]\n"
    "ds_read_b128 v[208:211], v20 offset:24576\n"
    "ds_read_b128 v[212:215], v20 offset:26624\n"
    "v_mfma_f32_16x16x32_bf16 v[76:79], v[44:47], v[204:207], v[76:79]\n"
    "ds_read_b64_tr_b16 v[28:29], v9 offset:40960\n"
    "ds_read_b64_tr_b16 v[30:31], v9 offset:41472\n"
    "v_mfma_f32_16x16x32_bf16 v[72:75], v[44:47], v[200:203], v[72:75]\n"
    "ds_read_b64_tr_b16 v[32:33], v10 offset:40960\n"
    "ds_read_b64_tr_b16 v[34:35], v10 offset:41472\n"
    "v_mfma_f32_16x16x32_bf16 v[80:83], v[48:51], v[200:203], v[80:83]\n"
    "ds_read_b64_tr_b16 v[36:37], v11 offset:40960\n"
    "ds_read_b64_tr_b16 v[38:39], v11 offset:41472\n"
    "v_mfma_f32_16x16x32_bf16 v[84:87], v[48:51], v[204:207], v[84:87]\n"
    "v_mfma_f32_16x16x32_bf16 v[92:95], v[52:55], v[204:207], v[92:95]\n"
    "v_mfma_f32_16x16x32_bf16 v[88:91], v[52:55], v[200:203], v[88:91]\n"
    "s_waitcnt lgkmcnt(0)\n"
    "v_mfma_f32_16x16x32_bf16 v[64:67], v[24:27], v[208:211], v[64:67]\n"
    "ds_read_b64_tr_b16 v[40:41], v8 offset:45056\n"
    "ds_read_b64_tr_b16 v[42:43], v8 offset:45568\n"
    "v_mfma_f32_16x16x32_bf16 v[68:71], v[24:27], v[212:215], v[68:71]\n"
    "ds_read_b128 v[216:219], v21 offset:24576\n"
    "ds_read_b128 v[220:223], v21 offset:26624\n"
    "v_mfma_f32_16x16x32_bf16 v[76:79], v[28:31], v[212:215], v[76:79]\n"
    "ds_read_b64_tr_b16 v[44:45], v9 offset:45056\n"
    "ds_read_b64_tr_b16 v[46:47], v9 offset:45568\n"
    "v_mfma_f32_16x16x32_bf16 v[72:75], v[28:31], v[208:211], v[72:75]\n"
    "ds_read_b64_tr_b16 v[48:49], v10 offset:45056\n"
    "ds_read_b64_tr_b16 v[50:51], v10 offset:45568\n"
    "v_mfma_f32_16x16x32_bf16 v[80:83], v[32:35], v[208:211], v[80:83]\n"
    "ds_read_b64_tr_b16 v[52:53], v11 offset:45056\n"
    "ds_read_b64_tr_b16 v[54:55], v11 offset:45568\n"
    "v_mfma_f32_16x16x32_bf16 v[84:87], v[32:35], v[212:215], v[84:87]\n"
    "v_mfma_f32_16x16x32_bf16 v[92:95], v[36:39], v[212:215], v[92:95]\n"
    "v_mfma_f32_16x16x32_bf16 v[88:91], v[36:39], v[208:211], v[88:91]\n"
    "s_waitcnt lgkmcnt(0)\n"
    "s_barrier\n"
    "v_mfma_f32_16x16x32_bf16 v[64:67], v[40:43], v[216:219], v[64:67]\n"
    "ds_read_b64_tr_b16 v[24:25], v12 offset:0\n"
    "ds_read_b64_tr_b16 v[26:27], v12 offset:512\n"
    "v_mfma_f32_16x16x32_bf16 v[68:71], v[40:43], v[220:223], v[68:71]\n"
    "ds_read_b128 v[224:227], v20 offset:49152\n"
    "ds_read_b128 v[228:231], v20 offset:51200\n"
    "v_mfma_f32_16x16x32_bf16 v[76:79], v[44:47], v[220:223], v[76:79]\n"
    "ds_read_b64_tr_b16 v[28:29], v13 offset:0\n"
    "ds_read_b64_tr_b16 v[30:31], v13 offset:512\n"
    "v_mfma_f32_16x16x32_bf16 v[72:75], v[44:47], v[216:219], v[72:75]\n"
    "ds_read_b64_tr_b16 v[32:33], v14 offset:0\n"
    "ds_read_b64_tr_b16 v[34:35], v14 offset:512\n"
    "v_mfma_f32_16x16x32_bf16 v[80:83], v[48:51], v[216:219], v[80:83]\n"
    "ds_read_b64_tr_b16 v[36:37], v15 offset:0\n"
    "ds_read_b64_tr_b16 v[38:39], v15 offset:512\n"
    "v_mfma_f32_16x16x32_bf16 v[84:87], v[48:51], v[220:223], v[84:87]\n"
    "v_mfma_f32_16x16x32_bf16 v[92:95], v[52:55], v[220:223], v[92:95]\n"
    "v_mfma_f32_16x16x32_bf16 v[88:91], v[52:55], v[216:219], v[88:91]\n"
    "s_waitcnt lgkmcnt(0)\n"
    "v_mfma_f32_16x16x32_bf16 v[64:67], v[24:27], v[224:227], v[64:67]\n"
    "ds_read_b64_tr_b16 v[40:41], v12 offset:4096\n"
    "ds_read_b64_tr_b16 v[42:43], v12 offset:4608\n"
    "v_mfma_f32_16x16x32_bf16 v[68:71], v[24:27], v[228:231], v[68:71]\n"
    "ds_read_b128 v[232:235], v21 offset:49152\n"
    "ds_read_b128 v[236:239], v21 offset:51200\n"
    "v_mfma_f32_16x16x32_bf16 v[76:79], v[28:31], v[228:231], v[76:79]\n"
    "ds_read_b64_tr_b16 v[44:45], v13 offset:4096\n"
    "ds_read_b64_tr_b16 v[46:47], v13 offset:4608\n"
    "v_mfma_f32_16x16x32_bf16 v[72:75], v[28:31], v[224:227], v[72:75]\n"
    "ds_read_b64_tr_b16 v[48:49], v14 offset:4096\n"
    "ds_read_b64_tr_b16 v[50:51], v14 offset:4608\n"
    "v_mfma_f32_16x16x32_bf16 v[80:83], v[32:35], v[224:227], v[80:83]\n"
    "ds_read_b64_tr_b16 v[52:53], v15 offset:4096\n"
    "ds_read_b64_tr_b16 v[54:55], v15 offset:4608\n"
    "v_mfma_f32_16x16x32_bf16 v[84:87], v[32:35], v[228:231], v[84:87]\n"
    "v_mfma_f32_16x16x32_bf16 v[92:95], v[36:39], v[228:231], v[92:95]\n"
    "v_mfma_f32_16x16x32_bf16 v[88:91], v[36:39], v[224:227], v[88:91]\n"
    "s_waitcnt lgkmcnt(0)\n"
    "s_barrier\n"
    "v_mfma_f32_16x16x32_bf16 v[64:67], v[40:43], v[232:235], v[64:67]\n"
    "ds_read_b64_tr_b16 v[24:25], v12 offset:24576\n"
    "ds_read_b64_tr_b16 v[26:27], v12 offset:25088\n"
    "v_mfma_f32_16x16x32_bf16 v[68:71], v[40:43], v[236:239], v[68:71]\n"
    "ds_read_b128 v[240:243], v22 offset:8192\n"
    "ds_read_b128 v[244:247], v22 offset:10240\n"
    "v_mfma_f32_16x16x32_bf16 v[76:79], v[44:47], v[236:239], v[76:79]\n"
    "ds_read_b64_tr_b16 v[28:29], v13 offset:24576\n"
    "ds_read_b64_tr_b16 v[30:31], v13 offset:25088\n"
    "v_mfma_f32_16x16x32_bf16 v[72:75], v[44:47], v[232:235], v[72:75]\n"
    "ds_read_b64_tr_b16 v[32:33], v14 offset:24576\n"
    "ds_read_b64_tr_b16 v[34:35], v14 offset:25088\n"
    "v_mfma_f32_16x16x32_bf16 v[80:83], v[48:51], v[232:235], v[80:83]\n"
    "ds_read_b64_tr_b16 v[36:37], v15 offset:24576\n"
    "ds_read_b64_tr_b16 v[38:39], v15 offset:25088\n"
    "v_mfma_f32_16x16x32_bf16 v[84:87], v[48:51], v[236:239], v[84:87]\n"
    "v_add_u32 v8, v6, v8\n"
    "v_add_u32 v9, v6, v9\n"
    "v_mfma_f32_16x16x32_bf16 v[92:95], v[52:55], v[236:239], v[92:95]\n"
    "v_add_u32 v10, v6, v10\n"
    "v_add_u32 v11, v6, v11\n"
    "v_mfma_f32_16x16x32_bf16 v[88:91], v[52:55], v[232:235], v[88:91]\n"
    "s_waitcnt lgkmcnt(0)\n"
    "v_mfma_f32_16x16x32_bf16 v[64:67], v[24:27], v[240:243], v[64:67]\n"
    "ds_read_b64_tr_b16 v[40:41], v12 offset:28672\n"
    "ds_read_b64_tr_b16 v[42:43], v12 offset:29184\n"
    "v_mfma_f32_16x16x32_bf16 v[68:71], v[24:27], v[244:247], v[68:71]\n"
    "ds_read_b128 v[248:251], v23 offset:8192\n"
    "ds_read_b128 v[252:255], v23 offset:10240\n"
    "v_mfma_f32_16x16x32_bf16 v[76:79], v[28:31], v[244:247], v[76:79]\n"
    "ds_read_b64_tr_b16 v[44:45], v13 offset:28672\n"
    "ds_read_b64_tr_b16 v[46:47], v13 offset:29184\n"
    "v_mfma_f32_16x16x32_bf16 v[72:75], v[28:31], v[240:243], v[72:75]\n"
    "ds_read_b64_tr_b16 v[48:49], v14 offset:28672\n"
    "ds_read_b64_tr_b16 v[50:51], v14 offset:29184\n"
    "v_mfma_f32_16x16x32_bf16 v[80:83], v[32:35], v[240:243], v[80:83]\n"
    "ds_read_b64_tr_b16 v[52:53], v15 offset:28672\n"
    "ds_read_b64_tr_b16 v[54:55], v15 offset:29184\n"
    "v_mfma_f32_16x16x32_bf16 v[84:87], v[32:35], v[244:247], v[84:87]\n"
    "v_mfma_f32_16x16x32_bf16 v[92:95], v[36:39], v[244:247], v[92:95]\n"
    "v_mfma_f32_16x16x32_bf16 v[88:91], v[36:39], v[240:243], v[88:91]\n"
    "s_waitcnt lgkmcnt(0)\n"
    "v_mfma_f32_16x16x32_bf16 v[64:67], v[40:43], v[248:251], v[64:67]\n"
    "v_add_u32 v20, s16, v4\n"
    "v_add_u32 v21, s17, v4\n"
    "v_mfma_f32_16x16x32_bf16 v[68:71], v[40:43], v[252:255], v[68:71]\n"
    "v_add_u32 v22, s18, v4\n"
    "v_add_u32 v23, s19, v4\n"
    "v_mfma_f32_16x16x32_bf16 v[76:79], v[44:47], v[252:255], v[76:79]\n"
    "v_mfma_f32_16x16x32_bf16 v[72:75], v[44:47], v[248:251], v[72:75]\n"
    "v_mfma_f32_16x16x32_bf16 v[80:83], v[48:51], v[248:251], v[80:83]\n"
    "v_mfma_f32_16x16x32_bf16 v[84:87], v[48:51], v[252:255], v[84:87]\n"
    "v_mfma_f32_16x16x32_bf16 v[92:95], v[52:55], v[252:255], v[92:95]\n"
    "v_mfma_f32_16x16x32_bf16 v[88:91], v[52:55], v[248:251], v[88:91]\n"
    "v_pk_mul_f32 v[56:57], v[64:65], s[52:53]\n"
    "v_pk_mul_f32 v[58:59], v[66:67], s[52:53]\n"
    "v_pk_mul_f32 v[60:61], v[72:73], s[52:53]\n"
    "v_pk_mul_f32 v[62:63], v[74:75], s[52:53]\n"
    "v_exp_f32 v56, v56\n"
    "v_exp_f32 v57, v57\n"
    "v_exp_f32 v58, v58\n"
    "v_exp_f32 v59, v59\n"
    "v_exp_f32 v60, v60\n"
    "v_exp_f32 v61, v61\n"
    "v_exp_f32 v62, v62\n"
    "v_exp_f32 v63, v63\n"
    "v_pk_add_f32 v[48:49], v[56:57], s[54:55]\n"
    "v_pk_add_f32 v[50:51], v[58:59], s[54:55]\n"
    "v_pk_add_f32 v[52:53], v[60:61], s[54:55]\n"
    "v_pk_add_f32 v[54:55], v[62:63], s[54:55]\n"
    "v_pk_fma_f32 v[56:57], v[56:57], v[56:57], v[48:49]\n"
    "v_pk_fma_f32 v[58:59], v[58:59], v[58:59], v[50:51]\n"
    "v_pk_fma_f32 v[60:61], v[60:61], v[60:61], v[52:53]\n"
    "v_pk_fma_f32 v[62:63], v[62:63], v[62:63], v[54:55]\n"
    "v_rcp_f32 v56, v56\n"
    "v_rcp_f32 v57, v57\n"
    "v_rcp_f32 v58, v58\n"
    "v_rcp_f32 v59, v59\n"
    "v_rcp_f32 v60, v60\n"
    "v_rcp_f32 v61, v61\n"
    "v_rcp_f32 v62, v62\n"
    "v_rcp_f32 v63, v63\n"
    "v_pk_mul_f32 v[48:49], v[48:49], v[56:57] clamp\n"
    "v_pk_mul_f32 v[50:51], v[50:51], v[58:59] clamp\n"
    "v_pk_mul_f32 v[52:53], v[52:53], v[60:61] clamp\n"
    "v_pk_mul_f32 v[54:55], v[54:55], v[62:63] clamp\n"
    "v_pk_mul_f32 v[48:49], v[64:65], v[48:49]\n"
    "v_pk_mul_f32 v[50:51], v[66:67], v[50:51]\n"
    "v_pk_mul_f32 v[52:53], v[72:73], v[52:53]\n"
    "v_pk_mul_f32 v[54:55], v[74:75], v[54:55]\n"
    "v_cvt_pk_bf16_f32 v64, v48, v49\n"
    "v_cvt_pk_bf16_f32 v65, v50, v51\n"
    "v_cvt_pk_bf16_f32 v66, v52, v53\n"
    "v_cvt_pk_bf16_f32 v67, v54, v55\n"
    "v_pk_mul_f32 v[56:57], v[68:69], s[52:53]\n"
    "v_pk_mul_f32 v[58:59], v[70:71], s[52:53]\n"
    "v_pk_mul_f32 v[60:61], v[76:77], s[52:53]\n"
    "v_pk_mul_f32 v[62:63], v[78:79], s[52:53]\n"
    "v_exp_f32 v56, v56\n"
    "v_exp_f32 v57, v57\n"
    "v_exp_f32 v58, v58\n"
    "v_exp_f32 v59, v59\n"
    "v_exp_f32 v60, v60\n"
    "v_exp_f32 v61, v61\n"
    "v_exp_f32 v62, v62\n"
    "v_exp_f32 v63, v63\n"
    "v_pk_add_f32 v[48:49], v[56:57], s[54:55]\n"
    "v_pk_add_f32 v[50:51], v[58:59], s[54:55]\n"
    "v_pk_add_f32 v[52:53], v[60:61], s[54:55]\n"
    "v_pk_add_f32 v[54:55], v[62:63], s[54:55]\n"
    "v_pk_fma_f32 v[56:57], v[56:57], v[56:57], v[48:49]\n"
    "v_pk_fma_f32 v[58:59], v[58:59], v[58:59], v[50:51]\n"
    "v_pk_fma_f32 v[60:61], v[60:61], v[60:61], v[52:53]\n"
    "v_pk_fma_f32 v[62:63], v[62:63], v[62:63], v[54:55]\n"
    "v_rcp_f32 v56, v56\n"
    "v_rcp_f32 v57, v57\n"
    "v_rcp_f32 v58, v58\n"
    "v_rcp_f32 v59, v59\n"
    "v_rcp_f32 v60, v60\n"
    "v_rcp_f32 v61, v61\n"
    "v_rcp_f32 v62, v62\n"
    "v_rcp_f32 v63, v63\n"
    "v_pk_mul_f32 v[48:49], v[48:49], v[56:57] clamp\n"
    "v_pk_mul_f32 v[50:51], v[50:51], v[58:59] clamp\n"
    "v_pk_mul_f32 v[52:53], v[52:53], v[60:61] clamp\n"
    "v_pk_mul_f32 v[54:55], v[54:55], v[62:63] clamp\n"
    "v_pk_mul_f32 v[48:49], v[68:69], v[48:49]\n"
    "v_pk_mul_f32 v[50:51], v[70:71], v[50:51]\n"
    "v_pk_mul_f32 v[52:53], v[76:77], v[52:53]\n"
    "v_pk_mul_f32 v[54:55], v[78:79], v[54:55]\n"
    "v_cvt_pk_bf16_f32 v68, v48, v49\n"
    "v_cvt_pk_bf16_f32 v69, v50, v51\n"
    "v_cvt_pk_bf16_f32 v70, v52, v53\n"
    "v_cvt_pk_bf16_f32 v71, v54, v55\n"
    "v_pk_mul_f32 v[56:57], v[80:81], s[52:53]\n"
    "v_pk_mul_f32 v[58:59], v[82:83], s[52:53]\n"
    "v_pk_mul_f32 v[60:61], v[88:89], s[52:53]\n"
    "v_pk_mul_f32 v[62:63], v[90:91], s[52:53]\n"
    "v_exp_f32 v56, v56\n"
    "L_pad0:\n"
    "s_cbranch_execz L_back0\n"
    "s_nop 0\n"
    "v_exp_f32 v57, v57\n"
    "v_exp_f32 v58, v58\n"
    "v_exp_f32 v59, v59\n"
    "v_exp_f32 v60, v60\n"
    "v_exp_f32 v61, v61\n"
    "v_exp_f32 v62, v62\n"
    "v_exp_f32 v63, v63\n"
    "v_pk_add_f32 v[48:49], v[56:57], s[54:55]\n"
    "v_pk_add_f32 v[50:51], v[58:59], s[54:55]\n"
    "v_pk_add_f32 v[52:53], v[60:61], s[54:55]\n"
    "v_pk_add_f32 v[54:55], v[62:63], s[54:55]\n"
    "v_pk_fma_f32 v[56:57], v[56:57], v[56:57], v[48:49]\n"
    "v_pk_fma_f32 v[58:59], v[58:59], v[58:59], v[50:51]\n"
    "v_pk_fma_f32 v[60:61], v[60:61], v[60:61], v[52:53]\n"
    "v_pk_fma_f32 v[62:63], v[62:63], v[62:63], v[54:55]\n"
    "v_rcp_f32 v56, v56\n"
    "v_rcp_f32 v57, v57\n"
    "v_rcp_f32 v58, v58\n"
    "v_rcp_f32 v59, v59\n"
    "v_rcp_f32 v60, v60\n"
    "v_rcp_f32 v61, v61\n"
    "v_rcp_f32 v62, v62\n"
    "v_rcp_f32 v63, v63\n"
    "v_pk_mul_f32 v[48:49], v[48:49], v[56:57] clamp\n"
    "v_pk_mul_f32 v[50:51], v[50:51], v[58:59] clamp\n"
    "v_pk_mul_f32 v[52:53], v[52:53], v[60:61] clamp\n"
    "v_pk_mul_f32 v[54:55], v[54:55], v[62:63] clamp\n"
    "v_pk_mul_f32 v[48:49], v[80:81], v[48:49]\n"
    "v_pk_mul_f32 v[50:51], v[82:83], v[50:51]\n"
    "v_pk_mul_f32 v[52:53], v[88:89], v[52:53]\n"
    "v_pk_mul_f32 v[54:55], v[90:91], v[54:55]\n"
    "v_cvt_pk_bf16_f32 v80, v48, v49\n"
    "v_cvt_pk_bf16_f32 v81, v50, v51\n"
    "v_cvt_pk_bf16_f32 v82, v52, v53\n"
    "v_cvt_pk_bf16_f32 v83, v54, v55\n"
    "s_barrier\n"
    "ds_read_b64_tr_b16 v[24:25], v12 offset:32768\n"
    "ds_read_b64_tr_b16 v[26:27], v12 offset:33280\n"
    "ds_read_b64_tr_b16 v[28:29], v13 offset:32768\n"
    "ds_read_b64_tr_b16 v[30:31], v13 offset:33280\n"
    "ds_read_b64_tr_b16 v[32:33], v14 offset:32768\n"
    "ds_read_b64_tr_b16 v[34:35], v14 offset:33280\n"
    "ds_read_b64_tr_b16 v[36:37], v15 offset:32768\n"
    "ds_read_b64_tr_b16 v[38:39], v15 offset:33280\n"
    "s_nop 0\n"
    "v_pk_mul_f32 v[56:57], v[84:85], s[52:53]\n"
    "v_pk_mul_f32 v[58:59], v[86:87], s[52:53]\n"
    "v_pk_mul_f32 v[60:61], v[92:93], s[52:53]\n"
    "v_pk_mul_f32 v[62:63], v[94:95], s[52:53]\n"
    "v_exp_f32 v56, v56\n"
    "v_exp_f32 v57, v57\n"
    "v_exp_f32 v58, v58\n"
    "v_exp_f32 v59, v59\n"
    "v_exp_f32 v60, v60\n"
    "v_exp_f32 v61, v61\n"
    "v_exp_f32 v62, v62\n"
    "v_exp_f32 v63, v63\n"
    "v_pk_add_f32 v[48:49], v[56:57], s[54:55]\n"
    "v_pk_add_f32 v[50:51], v[58:59], s[54:55]\n"
    "v_pk_add_f32 v[52:53], v[60:61], s[54:55]\n"
    "v_pk_add_f32 v[54:55], v[62:63], s[54:55]\n"
    "v_pk_fma_f32 v[56:57], v[56:57], v[56:57], v[48:49]\n"
    "v_pk_fma_f32 v[58:59], v[58:59], v[58:59], v[50:51]\n"
    "v_pk_fma_f32 v[60:61], v[60:61], v[60:61], v[52:53]\n"
    "v_pk_fma_f32 v[62:63], v[62:63], v[62:63], v[54:55]\n"
    "v_rcp_f32 v56, v56\n"
    "v_rcp_f32 v57, v57\n"
    "v_rcp_f32 v58, v58\n"
    "v_rcp_f32 v59, v59\n"
    "v_rcp_f32 v60, v60\n"
    "v_rcp_f32 v61, v61\n"
    "v_rcp_f32 v62, v62\n"
    "v_rcp_f32 v63, v63\n"
    "v_pk_mul_f32 v[48:49], v[48:49], v[56:57] clamp\n"
    "v_pk_mul_f32 v[50:51], v[50:51], v[58:59] clamp\n"
    "v_pk_mul_f32 v[52:53], v[52:53], v[60:61] clamp\n"
    "v_pk_mul_f32 v[54:55], v[54:55], v[62:63] clamp\n"
    "v_pk_mul_f32 v[48:49], v[84:85], v[48:49]\n"
    "v_pk_mul_f32 v[50:51], v[86:87], v[50:51]\n"
    "v_pk_mul_f32 v[52:53], v[92:93], v[52:53]\n"
    "v_pk_mul_f32 v[54:55], v[94:95], v[54:55]\n"
    "v_cvt_pk_bf16_f32 v84, v48, v49\n"
    "v_cvt_pk_bf16_f32 v85, v50, v51\n"
    "v_cvt_pk_bf16_f32 v86, v52, v53\n"
    "v_cvt_pk_bf16_f32 v87, v54, v55\n"
    "s_waitcnt lgkmcnt(6)\n"
    "v_mfma_f32_16x16x32_bf16 v[96:99], v[24:27], v[128:131], v[96:99]\n"
    "ds_read_b64_tr_b16 v[40:41], v12 offset:36864\n"
    "v_mfma_f32_16x16x32_bf16 v[100:103], v[24:27], v[132:135], v[100:103]\n"
    "ds_read_b64_tr_b16 v[42:43], v12 offset:37376\n"
    "s_waitcnt lgkmcnt(6)\n"
    "v_mfma_f32_16x16x32_bf16 v[108:111], v[28:31], v[132:135], v[108:111]\n"
    "ds_read_b64_tr_b16 v[44:45], v13 offset:36864\n"
    "v_mfma_f32_16x16x32_bf16 v[104:107], v[28:31], v[128:131], v[104:107]\n"
    "ds_read_b64_tr_b16 v[46:47], v13 offset:37376\n"
    "s_waitcnt lgkmcnt(6)\n"
    "v_mfma_f32_16x16x32_bf16 v[112:115], v[32:35], v[128:131], v[112:115]\n"
    "ds_read_b64_tr_b16 v[48:49], v14 offset:36864\n"
    "v_mfma_f32_16x16x32_bf16 v[116:119], v[32:35], v[132:135], v[116:119]\n"
    "ds_read_b64_tr_b16 v[50:51], v14 offset:37376\n"
    "s_waitcnt lgkmcnt(6)\n"
    "v_mfma_f32_16x16x32_bf16 v[124:127], v[36:39], v[132:135], v[124:127]\n"
    "ds_read_b64_tr_b16 v[52:53], v15 offset:36864\n"
    "v_mfma_f32_16x16x32_bf16 v[120:123], v[36:39], v[128:131], v[120:123]\n"
    "ds_read_b64_tr_b16 v[54:55], v15 offset:37376\n"
    "s_waitcnt lgkmcnt(6)\n"
    "v_mfma_f32_16x16x32_bf16 v[96:99], v[40:43], v[136:139], v[96:99]\n"
    "ds_read_b64_tr_b16 v[24:25], v12 offset:40960\n"
    "v_mfma_f32_16x16x32_bf16 v[100:103], v[40:43], v[140:143], v[100:103]\n"
    "ds_read_b64_tr_b16 v[26:27], v12 offset:41472\n"
    "s_waitcnt lgkmcnt(6)\n"
    "v_mfma_f32_16x16x32_bf16 v[108:111], v[44:47], v[140:143], v[108:111]\n"
    "ds_read_b64_tr_b16 v[28:29], v13 offset:40960\n"
    "v_mfma_f32_16x16x32_bf16 v[104:107], v[44:47], v[136:139], v[104:107]\n"
    "ds_read_b64_tr_b16 v[30:31], v13 offset:41472\n"
    "s_waitcnt lgkmcnt(6)\n"
    "v_mfma_f32_16x16x32_bf16 v[112:115], v[48:51], v[136:139], v[112:115]\n"
    "ds_read_b64_tr_b16 v[32:33], v14 offset:40960\n"
    "v_mfma_f32_16x16x32_bf16 v[116:119], v[48:51], v[140:143], v[116:119]\n"
    "ds_read_b64_tr_b16 v[34:35], v14 offset:41472\n"
    "s_waitcnt lgkmcnt(6)\n"
    "v_mfma_f32_16x16x32_bf16 v[124:127], v[52:55], v[140:143], v[124:127]\n"
    "ds_read_b64_tr_b16 v[36:37], v15 offset:40960\n"
    "v_mfma_f32_16x16x32_bf16 v[120:123], v[52:55], v[136:139], v[120:123]\n"
    "ds_read_b64_tr_b16 v[38:39], v15 offset:41472\n"
    "s_waitcnt lgkmcnt(6)\n"
    "v_mfma_f32_16x16x32_bf16 v[96:99], v[24:27], v[144:147], v[96:99]\n"
    "ds_read_b64_tr_b16 v[40:41], v12 offset:45056\n"
    "v_mfma_f32_16x16x32_bf16 v[100:103], v[24:27], v[148:151], v[100:103]\n"
    "ds_read_b64_tr_b16 v[42:43], v12 offset:45568\n"
    "s_waitcnt lgkmcnt(6)\n"
    "v_mfma_f32_16x16x32_bf16 v[108:111], v[28:31], v[148:151], v[108:111]\n"
    "ds_read_b64_tr_b16 v[44:45], v13 offset:45056\n"
    "v_mfma_f32_16x16x32_bf16 v[104:107], v[28:31], v[144:147], v[104:107]\n"
    "ds_read_b64_tr_b16 v[46:47], v13 offset:45568\n"
    "s_waitcnt lgkmcnt(6)\n"
    "v_mfma_f32_16x16x32_bf16 v[112:115], v[32:35], v[144:147], v[112:115]\n"
    "ds_read_b64_tr_b16 v[48:49], v14 offset:45056\n"
    "v_mfma_f32_16x16x32_bf16 v[116:119], v[32:35], v[148:151], v[116:119]\n"
    "ds_read_b64_tr_b16 v[50:51], v14 offset:45568\n"
    "s_waitcnt lgkmcnt(6)\n"
    "v_mfma_f32_16x16x32_bf16 v[124:127], v[36:39], v[148:151], v[124:127]\n"
    "ds_read_b64_tr_b16 v[52:53], v15 offset:45056\n"
    "v_mfma_f32_16x16x32_bf16 v[120:123], v[36:39], v[144:147], v[120:123]\n"
    "ds_read_b64_tr_b16 v[54:55], v15 offset:45568\n"
    "s_waitcnt lgkmcnt(6)\n"
    "v_mfma_f32_16x16x32_bf16 v[96:99], v[40:43], v[152:155], v[96:99]\n"
    "ds_read_b64_tr_b16 v[24:25], v12 offset:49152\n"
    "v_mfma_f32_16x16x32_bf16 v[100:103], v[40:43], v[156:159], v[100:103]\n"
    "ds_read_b64_tr_b16 v[26:27], v12 offset:49664\n"
    "buffer_store_dwordx4 v[64:67], v4, s[28:31], 0 offen offset:0 sc1\n"
    "s_waitcnt lgkmcnt(6)\n"
    "v_mfma_f32_16x16x32_bf16 v[108:111], v[44:47], v[156:159], v[108:111]\n"
    "ds_read_b64_tr_b16 v[28:29], v13 offset:49152\n"
    "v_mfma_f32_16x16x32_bf16 v[104:107], v[44:47], v[152:155], v[104:107]\n"
    "ds_read_b64_tr_b16 v[30:31], v13 offset:49664\n"
    "buffer_store_dwordx4 v[68:71], v4, s[28:31], s46 offen offset:0 sc1\n"
    "s_waitcnt lgkmcnt(6)\n"
    "v_mfma_f32_16x16x32_bf16 v[112:115], v[48:51], v[152:155], v[112:115]\n"
    "ds_read_b64_tr_b16 v[32:33], v14 offset:49152\n"
    "v_mfma_f32_16x16x32_bf16 v[116:119], v[48:51], v[156:159], v[116:119]\n"
    "ds_read_b64_tr_b16 v[34:35], v14 offset:49664\n"
    "buffer_store_dwordx4 v[80:83], v4, s[28:31], 0 offen offset:2048 sc1\n"
    "s_waitcnt lgkmcnt(6)\n"
    "v_mfma_f32_16x16x32_bf16 v[124:127], v[52:55], v[156:159], v[124:127]\n"
    "ds_read_b64_tr_b16 v[36:37], v15 offset:49152\n"
    "v_mfma_f32_16x16x32_bf16 v[120:123], v[52:55], v[152:155], v[120:123]\n"
    "ds_read_b64_tr_b16 v[38:39], v15 offset:49664\n"
    "buffer_store_dwordx4 v[84:87], v4, s[28:31], s46 offen offset:2048 sc1\n"
    "s_waitcnt lgkmcnt(6)\n"
    "v_mfma_f32_16x16x32_bf16 v[96:99], v[24:27], v[160:163], v[96:99]\n"
    "ds_read_b64_tr_b16 v[40:41], v12 offset:53248\n"
    "v_mfma_f32_16x16x32_bf16 v[100:103], v[24:27], v[164:167], v[100:103]\n"
    "ds_read_b64_tr_b16 v[42:43], v12 offset:53760\n"
    "s_waitcnt lgkmcnt(6)\n"
    "v_mfma_f32_16x16x32_bf16 v[108:111], v[28:31], v[164:167], v[108:111]\n"
    "ds_read_b64_tr_b16 v[44:45], v13 offset:53248\n"
    "v_mfma_f32_16x16x32_bf16 v[104:107], v[28:31], v[160:163], v[104:107]\n"
    "ds_read_b64_tr_b16 v[46:47], v13 offset:53760\n"
    "s_waitcnt lgkmcnt(6)\n"
    "v_mfma_f32_16x16x32_bf16 v[112:115], v[32:35], v[160:163], v[112:115]\n"
    "ds_read_b64_tr_b16 v[48:49], v14 offset:53248\n"
    "v_mfma_f32_16x16x32_bf16 v[116:119], v[32:35], v[164:167], v[116:119]\n"
    "ds_read_b64_tr_b16 v[50:51], v14 offset:53760\n"
    "s_waitcnt lgkmcnt(6)\n"
    "v_mfma_f32_16x16x32_bf16 v[124:127], v[36:39], v[164:167], v[124:127]\n"
    "ds_read_b64_tr_b16 v[52:53], v15 offset:53248\n"
    "v_mfma_f32_16x16x32_bf16 v[120:123], v[36:39], v[160:163], v[120:123]\n"
    "ds_read_b64_tr_b16 v[54:55], v15 offset:53760\n"
    "s_waitcnt lgkmcnt(6)\n"
    "v_mfma_f32_16x16x32_bf16 v[96:99], v[40:43], v[168:171], v[96:99]\n"
    "ds_read_b64_tr_b16 v[24:25], v12 offset:57344\n"
    "v_mfma_f32_16x16x32_bf16 v[100:103], v[40:43], v[172:175], v[100:103]\n"
    "ds_read_b64_tr_b16 v[26:27], v12 offset:57856\n"
    "s_waitcnt lgkmcnt(6)\n"
    "v_mfma_f32_16x16x32_bf16 v[108:111], v[44:47], v[172:175], v[108:111]\n"
    "ds_read_b64_tr_b16 v[28:29], v13 offset:57344\n"
    "v_mfma_f32_16x16x32_bf16 v[104:107], v[44:47], v[168:171], v[104:107]\n"
    "ds_read_b64_tr_b16 v[30:31], v13 offset:57856\n"
    "s_waitcnt lgkmcnt(6)\n"
    "v_mfma_f32_16x16x32_bf16 v[112:115], v[48:51], v[168:171], v[112:115]\n"
    "ds_read_b64_tr_b16 v[32:33], v14 offset:57344\n"
    "v_mfma_f32_16x16x32_bf16 v[116:119], v[48:51], v[172:175], v[116:119]\n"
    "ds_read_b64_tr_b16 v[34:35], v14 offset:57856\n"
    "s_waitcnt lgkmcnt(6)\n"
    "v_mfma_f32_16x16x32_bf16 v[124:127], v[52:55], v[172:175], v[124:127]\n"
    "ds_read_b64_tr_b16 v[36:37], v15 offset:57344\n"
    "v_mfma_f32_16x16x32_bf16 v[120:123], v[52:55], v[168:171], v[120:123]\n"
    "ds_read_b64_tr_b16 v[38:39], v15 offset:57856\n"
    "s_waitcnt lgkmcnt(6)\n"
    "v_mfma_f32_16x16x32_bf16 v[96:99], v[24:27], v[176:179], v[96:99]\n"
    "ds_read_b64_tr_b16 v[40:41], v12 offset:61440\n"
    "v_mfma_f32_16x16x32_bf16 v[100:103], v[24:27], v[180:183], v[100:103]\n"
    "ds_read_b64_tr_b16 v[42:43], v12 offset:61952\n"
    "s_waitcnt lgkmcnt(6)\n"
    "v_mfma_f32_16x16x32_bf16 v[108:111], v[28:31], v[180:183], v[108:111]\n"
    "ds_read_b64_tr_b16 v[44:45], v13 offset:61440\n"
    "v_mfma_f32_16x16x32_bf16 v[104:107], v[28:31], v[176:179], v[104:107]\n"
    "ds_read_b64_tr_b16 v[46:47], v13 offset:61952\n"
    "s_waitcnt lgkmcnt(6)\n"
    "v_mfma_f32_16x16x32_bf16 v[112:115], v[32:35], v[176:179], v[112:115]\n"
    "ds_read_b64_tr_b16 v[48:49], v14 offset:61440\n"
    "v_mfma_f32_16x16x32_bf16 v[116:119], v[32:35], v[180:183], v[116:119]\n"
    "ds_read_b64_tr_b16 v[50:51], v14 offset:61952\n"
    "s_waitcnt lgkmcnt(6)\n"
    "v_mfma_f32_16x16x32_bf16 v[124:127], v[36:39], v[180:183], v[124:127]\n"
    "ds_read_b64_tr_b16 v[52:53], v15 offset:61440\n"
    "v_mfma_f32_16x16x32_bf16 v[120:123], v[36:39], v[176:179], v[120:123]\n"
    "ds_read_b64_tr_b16 v[54:55], v15 offset:61952\n"
    "s_waitcnt lgkmcnt(6)\n"
    "v_mfma_f32_16x16x32_bf16 v[96:99], v[40:43], v[184:187], v[96:99]\n"
    "ds_read_b64_tr_b16 v[24:25], v16 offset:0\n"
    "v_mfma_f32_16x16x32_bf16 v[100:103], v[40:43], v[188:191], v[100:103]\n"
    "ds_read_b64_tr_b16 v[26:27], v16 offset:512\n"
    "s_waitcnt lgkmcnt(6)\n"
    "v_mfma_f32_16x16x32_bf16 v[108:111], v[44:47], v[188:191], v[108:111]\n"
    "ds_read_b64_tr_b16 v[28:29], v17 offset:0\n"
    "v_mfma_f32_16x16x32_bf16 v[104:107], v[44:47], v[184:187], v[104:107]\n"
    "ds_read_b64_tr_b16 v[30:31], v17 offset:512\n"
    "s_waitcnt lgkmcnt(6)\n"
    "v_mfma_f32_16x16x32_bf16 v[112:115], v[48:51], v[184:187], v[112:115]\n"
    "ds_read_b64_tr_b16 v[32:33], v18 offset:0\n"
    "v_mfma_f32_16x16x32_bf16 v[116:119], v[48:51], v[188:191], v[116:119]\n"
    "ds_read_b64_tr_b16 v[34:35], v18 offset:512\n"
    "s_waitcnt lgkmcnt(6)\n"
    "v_mfma_f32_16x16x32_bf16 v[124:127], v[52:55], v[188:191], v[124:127]\n"
    "ds_read_b64_tr_b16 v[36:37], v19 offset:0\n"
    "v_mfma_f32_16x16x32_bf16 v[120:123], v[52:55], v[184:187], v[120:123]\n"
    "ds_read_b64_tr_b16 v[38:39], v19 offset:512\n"
    "s_waitcnt lgkmcnt(6)\n"
    "s_barrier\n"
    "v_mfma_f32_16x16x32_bf16 v[96:99], v[24:27], v[192:195], v[96:99]\n"
    "ds_read_b64_tr_b16 v[40:41], v16 offset:4096\n"
    "ds_read_b64_tr_b16 v[128:129], v8 offset:0\n"
    "v_mfma_f32_16x16x32_bf16 v[100:103], v[24:27], v[196:199], v[100:103]\n"
    "ds_read_b64_tr_b16 v[42:43], v16 offset:4608\n"
    "ds_read_b64_tr_b16 v[130:131], v8 offset:2048\n"
    "s_waitcnt lgkmcnt(8)\n"
    "v_mfma_f32_16x16x32_bf16 v[108:111], v[28:31], v[196:199], v[108:111]\n"
    "ds_read_b64_tr_b16 v[44:45], v17 offset:4096\n"
    "ds_read_b64_tr_b16 v[132:133], v9 offset:0\n"
    "v_mfma_f32_16x16x32_bf16 v[104:107], v[28:31], v[192:195], v[104:107]\n"
    "ds_read_b64_tr_b16 v[46:47], v17 offset:4608\n"
    "s_waitcnt lgkmcnt(9)\n"
    "v_mfma_f32_16x16x32_bf16 v[112:115], v[32:35], v[192:195], v[112:115]\n"
    "ds_read_b64_tr_b16 v[48:49], v18 offset:4096\n"
    "ds_read_b64_tr_b16 v[134:135], v9 offset:2048\n"
    "v_mfma_f32_16x16x32_bf16 v[116:119], v[32:35], v[196:199], v[116:119]\n"
    "ds_read_b64_tr_b16 v[50:51], v18 offset:4608\n"
    "s_waitcnt lgkmcnt(10)\n"
    "v_mfma_f32_16x16x32_bf16 v[124:127], v[36:39], v[196:199], v[124:127]\n"
    "ds_read_b64_tr_b16 v[52:53], v19 offset:4096\n"
    "ds_read_b64_tr_b16 v[136:137], v10 offset:0\n"
    "v_mfma_f32_16x16x32_bf16 v[120:123], v[36:39], v[192:195], v[120:123]\n"
    "ds_read_b64_tr_b16 v[54:55], v19 offset:4608\n"
    "s_waitcnt lgkmcnt(10)\n"
    "v_mfma_f32_16x16x32_bf16 v[96:99], v[40:43], v[200:203], v[96:99]\n"
    "ds_read_b64_tr_b16 v[24:25], v16 offset:8192\n"
    "ds_read_b64_tr_b16 v[138:139], v10 offset:2048\n"
    "v_mfma_f32_16x16x32_bf16 v[100:103], v[40:43], v[204:207], v[100:103]\n"
    "ds_read_b64_tr_b16 v[26:27], v16 offset:8704\n"
    "ds_read_b64_tr_b16 v[140:141], v11 offset:0\n"
    "s_waitcnt lgkmcnt(10)\n"
    "v_mfma_f32_16x16x32_bf16 v[108:111], v[44:47], v[204:207], v[108:111]\n"
    "ds_read_b64_tr_b16 v[28:29], v17 offset:8192\n"
    "ds_read_b64_tr_b16 v[142:143], v11 offset:2048\n"
    "v_mfma_f32_16x16x32_bf16 v[104:107], v[44:47], v[200:203], v[104:107]\n"
    "ds_read_b64_tr_b16 v[30:31], v17 offset:8704\n"
    "s_waitcnt lgkmcnt(10)\n"
    "v_mfma_f32_16x16x32_bf16 v[112:115], v[48:51], v[200:203], v[112:115]\n"
    "ds_read_b64_tr_b16 v[32:33], v18 offset:8192\n"
    "ds_read_b64_tr_b16 v[144:145], v8 offset:16384\n"
    "v_mfma_f32_16x16x32_bf16 v[116:119], v[48:51], v[204:207], v[116:119]\n"
    "ds_read_b64_tr_b16 v[34:35], v18 offset:8704\n"
    "s_waitcnt lgkmcnt(10)\n"
    "v_mfma_f32_16x16x32_bf16 v[124:127], v[52:55], v[204:207], v[124:127]\n"
    "ds_read_b64_tr_b16 v[36:37], v19 offset:8192\n"
    "ds_read_b64_tr_b16 v[146:147], v8 offset:18432\n"
    "v_mfma_f32_16x16x32_bf16 v[120:123], v[52:55], v[200:203], v[120:123]\n"
    "ds_read_b64_tr_b16 v[38:39], v19 offset:8704\n"
    "s_waitcnt lgkmcnt(10)\n"
    "v_mfma_f32_16x16x32_bf16 v[96:99], v[24:27], v[208:211], v[96:99]\n"
    "ds_read_b64_tr_b16 v[40:41], v16 offset:12288\n"
    "ds_read_b64_tr_b16 v[148:149], v9 offset:16384\n"
    "v_mfma_f32_16x16x32_bf16 v[100:103], v[24:27], v[212:215], v[100:103]\n"
    "ds_read_b64_tr_b16 v[42:43], v16 offset:12800\n"
    "ds_read_b64_tr_b16 v[150:151], v9 offset:18432\n"
    "s_waitcnt lgkmcnt(10)\n"
    "v_mfma_f32_16x16x32_bf16 v[108:111], v[28:31], v[212:215], v[108:111]\n"
    "ds_read_b64_tr_b16 v[44:45], v17 offset:12288\n"
    "ds_read_b64_tr_b16 v[152:153], v10 offset:16384\n"
    "v_mfma_f32_16x16x32_bf16 v[104:107], v[28:31], v[208:211], v[104:107]\n"
    "ds_read_b64_tr_b16 v[46:47], v17 offset:12800\n"
    "s_waitcnt lgkmcnt(10)\n"
    "v_mfma_f32_16x16x32_bf16 v[112:115], v[32:35], v[208:211], v[112:115]\n"
    "ds_read_b64_tr_b16 v[48:49], v18 offset:12288\n"
    "ds_read_b64_tr_b16 v[154:155], v10 offset:18432\n"
    "v_mfma_f32_16x16x32_bf16 v[116:119], v[32:35], v[212:215], v[116:119]\n"
    "ds_read_b64_tr_b16 v[50:51], v18 offset:12800\n"
    "s_waitcnt lgkmcnt(10)\n"
    "v_mfma_f32_16x16x32_bf16 v[124:127], v[36:39], v[212:215], v[124:127]\n"
    "ds_read_b64_tr_b16 v[52:53], v19 offset:12288\n"
    "ds_read_b64_tr_b16 v[156:157], v11 offset:16384\n"
    "v_mfma_f32_16x16x32_bf16 v[120:123], v[36:39], v[208:211], v[120:123]\n"
    "ds_read_b64_tr_b16 v[54:55], v19 offset:12800\n"
    "s_waitcnt lgkmcnt(10)\n"
    "v_mfma_f32_16x16x32_bf16 v[96:99], v[40:43], v[216:219], v[96:99]\n"
    "ds_read_b64_tr_b16 v[24:25], v16 offset:16384\n"
    "ds_read_b64_tr_b16 v[158:159], v11 offset:18432\n"
    "v_mfma_f32_16x16x32_bf16 v[100:103], v[40:43], v[220:223], v[100:103]\n"
    "ds_read_b64_tr_b16 v[26:27], v16 offset:16896\n"
    "ds_read_b64_tr_b16 v[160:161], v8 offset:32768\n"
    "s_waitcnt lgkmcnt(10)\n"
    "v_mfma_f32_16x16x32_bf16 v[108:111], v[44:47], v[220:223], v[108:111]\n"
    "ds_read_b64_tr_b16 v[28:29], v17 offset:16384\n"
    "ds_read_b64_tr_b16 v[162:163], v8 offset:34816\n"
    "v_mfma_f32_16x16x32_bf16 v[104:107], v[44:47], v[216:219], v[104:107]\n"
    "ds_read_b64_tr_b16 v[30:31], v17 offset:16896\n"
    "s_waitcnt lgkmcnt(10)\n"
    "v_mfma_f32_16x16x32_bf16 v[112:115], v[48:51], v[216:219], v[112:115]\n"
    "ds_read_b64_tr_b16 v[32:33], v18 offset:16384\n"
    "ds_read_b64_tr_b16 v[164:165], v9 offset:32768\n"
    "v_mfma_f32_16x16x32_bf16 v[116:119], v[48:51], v[220:223], v[116:119]\n"
    "ds_read_b64_tr_b16 v[34:35], v18 offset:16896\n"
    "s_waitcnt lgkmcnt(10)\n"
    "v_mfma_f32_16x16x32_bf16 v[124:127], v[52:55], v[220:223], v[124:127]\n"
    "ds_read_b64_tr_b16 v[36:37], v19 offset:16384\n"
    "ds_read_b64_tr_b16 v[166:167], v9 offset:34816\n"
    "v_mfma_f32_16x16x32_bf16 v[120:123], v[52:55], v[216:219], v[120:123]\n"
    "ds_read_b64_tr_b16 v[38:39], v19 offset:16896\n"
    "s_waitcnt lgkmcnt(10)\n"
    "s_waitcnt vmcnt(0)\n"
    "s_barrier\n"
    "v_mfma_f32_16x16x32_bf16 v[96:99], v[24:27], v[224:227], v[96:99]\n"
    "ds_read_b64_tr_b16 v[40:41], v16 offset:20480\n"
    "ds_read_b64_tr_b16 v[168:169], v10 offset:32768\n"
    "v_mfma_f32_16x16x32_bf16 v[100:103], v[24:27], v[228:231], v[100:103]\n"
    "ds_read_b64_tr_b16 v[42:43], v16 offset:20992\n"
    "ds_read_b64_tr_b16 v[170:171], v10 offset:34816\n"
    "s_waitcnt lgkmcnt(10)\n"
    "v_mfma_f32_16x16x32_bf16 v[108:111], v[28:31], v[228:231], v[108:111]\n"
    "ds_read_b64_tr_b16 v[44:45], v17 offset:20480\n"
    "ds_read_b64_tr_b16 v[172:173], v11 offset:32768\n"
    "v_mfma_f32_16x16x32_bf16 v[104:107], v[28:31], v[224:227], v[104:107]\n"
    "ds_read_b64_tr_b16 v[46:47], v17 offset:20992\n"
    "s_waitcnt lgkmcnt(10)\n"
    "v_mfma_f32_16x16x32_bf16 v[112:115], v[32:35], v[224:227], v[112:115]\n"
    "ds_read_b64_tr_b16 v[48:49], v18 offset:20480\n"
    "ds_read_b64_tr_b16 v[174:175], v11 offset:34816\n"
    "v_mfma_f32_16x16x32_bf16 v[116:119], v[32:35], v[228:231], v[116:119]\n"
    "ds_read_b64_tr_b16 v[50:51], v18 offset:20992\n"
    "s_waitcnt lgkmcnt(10)\n"
    "v_mfma_f32_16x16x32_bf16 v[124:127], v[36:39], v[228:231], v[124:127]\n"
    "ds_read_b64_tr_b16 v[52:53], v19 offset:20480\n"
    "ds_read_b64_tr_b16 v[176:177], v8 offset:49152\n"
    "v_mfma_f32_16x16x32_bf16 v[120:123], v[36:39], v[224:227], v[120:123]\n"
    "ds_read_b64_tr_b16 v[54:55], v19 offset:20992\n"
    "s_waitcnt lgkmcnt(10)\n"
    "v_mfma_f32_16x16x32_bf16 v[96:99], v[40:43], v[232:235], v[96:99]\n"
    "ds_read_b64_tr_b16 v[24:25], v16 offset:24576\n"
    "ds_read_b64_tr_b16 v[178:179], v8 offset:51200\n"
    "v_mfma_f32_16x16x32_bf16 v[100:103], v[40:43], v[236:239], v[100:103]\n"
    "ds_read_b64_tr_b16 v[26:27], v16 offset:25088\n"
    "ds_read_b64_tr_b16 v[180:181], v9 offset:49152\n"
    "s_waitcnt lgkmcnt(10)\n"
    "v_mfma_f32_16x16x32_bf16 v[108:111], v[44:47], v[236:239], v[108:111]\n"
    "ds_read_b64_tr_b16 v[28:29], v17 offset:24576\n"
    "ds_read_b64_tr_b16 v[182:183], v9 offset:51200\n"
    "v_mfma_f32_16x16x32_bf16 v[104:107], v[44:47], v[232:235], v[104:107]\n"
    "ds_read_b64_tr_b16 v[30:31], v17 offset:25088\n"
    "s_waitcnt lgkmcnt(10)\n"
    "v_mfma_f32_16x16x32_bf16 v[112:115], v[48:51], v[232:235], v[112:115]\n"
    "ds_read_b64_tr_b16 v[32:33], v18 offset:24576\n"
    "ds_read_b64_tr_b16 v[184:185], v10 offset:49152\n"
    "v_mfma_f32_16x16x32_bf16 v[116:119], v[48:51], v[236:239], v[116:119]\n"
    "ds_read_b64_tr_b16 v[34:35], v18 offset:25088\n"
    "s_waitcnt lgkmcnt(10)\n"
    "v_mfma_f32_16x16x32_bf16 v[124:127], v[52:55], v[236:239], v[124:127]\n"
    "ds_read_b64_tr_b16 v[36:37], v19 offset:24576\n"
    "ds_read_b64_tr_b16 v[186:187], v10 offset:51200\n"
    "v_mfma_f32_16x16x32_bf16 v[120:123], v[52:55], v[232:235], v[120:123]\n"
    "ds_read_b64_tr_b16 v[38:39], v19 offset:25088\n"
    "s_waitcnt lgkmcnt(10)\n"
    "v_mfma_f32_16x16x32_bf16 v[96:99], v[24:27], v[240:243], v[96:99]\n"
    "ds_read_b64_tr_b16 v[40:41], v16 offset:28672\n"
    "ds_read_b64_tr_b16 v[188:189], v11 offset:49152\n"
    "v_mfma_f32_16x16x32_bf16 v[100:103], v[24:27], v[244:247], v[100:103]\n"
    "ds_read_b64_tr_b16 v[42:43], v16 offset:29184\n"
    "ds_read_b64_tr_b16 v[190:191], v11 offset:51200\n"
    "s_waitcnt lgkmcnt(10)\n"
    "v_mfma_f32_16x16x32_bf16 v[108:111], v[28:31], v[244:247], v[108:111]\n"
    "ds_read_b64_tr_b16 v[44:45], v17 offset:28672\n"
    "v_mfma_f32_16x16x32_bf16 v[104:107], v[28:31], v[240:243], v[104:107]\n"
    "ds_read_b64_tr_b16 v[46:47], v17 offset:29184\n"
    "s_waitcnt lgkmcnt(9)\n"
    "v_mfma_f32_16x16x32_bf16 v[112:115], v[32:35], v[240:243], v[112:115]\n"
    "ds_read_b64_tr_b16 v[48:49], v18 offset:28672\n"
    "v_mfma_f32_16x16x32_bf16 v[116:119], v[32:35], v[244:247], v[116:119]\n"
    "ds_read_b64_tr_b16 v[50:51], v18 offset:29184\n"
    "s_waitcnt lgkmcnt(8)\n"
    "v_mfma_f32_16x16x32_bf16 v[124:127], v[36:39], v[244:247], v[124:127]\n"
    "ds_read_b64_tr_b16 v[52:53], v19 offset:28672\n"
    "v_mfma_f32_16x16x32_bf16 v[120:123], v[36:39], v[240:243], v[120:123]\n"
    "ds_read_b64_tr_b16 v[54:55], v19 offset:29184\n"
    "s_waitcnt lgkmcnt(7)\n"
    "v_mfma_f32_16x16x32_bf16 v[96:99], v[40:43], v[248:251], v[96:99]\n"
    "v_mfma_f32_16x16x32_bf16 v[100:103], v[40:43], v[252:255], v[100:103]\n"
    "s_waitcnt lgkmcnt(4)\n"
    "v_mfma_f32_16x16x32_bf16 v[108:111], v[44:47], v[252:255], v[108:111]\n"
    "v_mfma_f32_16x16x32_bf16 v[104:107], v[44:47], v[248:251], v[104:107]\n"
    "s_waitcnt lgkmcnt(2)\n"
    "v_mfma_f32_16x16x32_bf16 v[112:115], v[48:51], v[248:251], v[112:115]\n"
    "v_mfma_f32_16x16x32_bf16 v[116:119], v[48:51], v[252:255], v[116:119]\n"
    "s_waitcnt lgkmcnt(0)\n"
    "s_barrier\n"
    "v_mfma_f32_16x16x32_bf16 v[124:127], v[52:55], v[252:255], v[124:127]\n"
    "v_mfma_f32_16x16x32_bf16 v[120:123], v[52:55], v[248:251], v[120:123]\n"
    "v_pk_mul_f32 v[56:57], v[96:97], s[52:53]\n"
    "v_pk_mul_f32 v[58:59], v[98:99], s[52:53]\n"
    "v_pk_mul_f32 v[60:61], v[104:105], s[52:53]\n"
    "v_pk_mul_f32 v[62:63], v[106:107], s[52:53]\n"
    "v_exp_f32 v56, v56\n"
    "v_exp_f32 v57, v57\n"
    "v_exp_f32 v58, v58\n"
    "v_exp_f32 v59, v59\n"
    "v_exp_f32 v60, v60\n"
    "v_exp_f32 v61, v61\n"
    "v_exp_f32 v62, v62\n"
    "v_exp_f32 v63, v63\n"
    "v_pk_add_f32 v[48:49], v[56:57], s[54:55]\n"
    "v_pk_add_f32 v[50:51], v[58:59], s[54:55]\n"
    "v_pk_add_f32 v[52:53], v[60:61], s[54:55]\n"
    "v_pk_add_f32 v[54:55], v[62:63], s[54:55]\n"
    "v_pk_fma_f32 v[56:57], v[56:57], v[56:57], v[48:49]\n"
    "v_pk_fma_f32 v[58:59], v[58:59], v[58:59], v[50:51]\n"
    "v_pk_fma_f32 v[60:61], v[60:61], v[60:61], v[52:53]\n"
    "v_pk_fma_f32 v[62:63], v[62:63], v[62:63], v[54:55]\n"
    "v_rcp_f32 v56, v56\n"
    "v_rcp_f32 v57, v57\n"
    "v_rcp_f32 v58, v58\n"
    "v_rcp_f32 v59, v59\n"
    "v_rcp_f32 v60, v60\n"
    "v_rcp_f32 v61, v61\n"
    "v_rcp_f32 v62, v62\n"
    "v_rcp_f32 v63, v63\n"
    "v_pk_mul_f32 v[48:49], v[48:49], v[56:57] clamp\n"
    "v_pk_mul_f32 v[50:51], v[50:51], v[58:59] clamp\n"
    "v_pk_mul_f32 v[52:53], v[52:53], v[60:61] clamp\n"
    "v_pk_mul_f32 v[54:55], v[54:55], v[62:63] clamp\n"
    "v_pk_mul_f32 v[48:49], v[96:97], v[48:49]\n"
    "v_pk_mul_f32 v[50:51], v[98:99], v[50:51]\n"
    "v_pk_mul_f32 v[52:53], v[104:105], v[52:53]\n"
    "v_pk_mul_f32 v[54:55], v[106:107], v[54:55]\n"
    "v_cvt_pk_bf16_f32 v96, v48, v49\n"
    "v_cvt_pk_bf16_f32 v97, v50, v51\n"
    "v_cvt_pk_bf16_f32 v98, v52, v53\n"
    "v_cvt_pk_bf16_f32 v99, v54, v55\n"
    "v_pk_mul_f32 v[56:57], v[100:101], s[52:53]\n"
    "v_pk_mul_f32 v[58:59], v[102:103], s[52:53]\n"
    "v_pk_mul_f32 v[60:61], v[108:109], s[52:53]\n"
    "v_pk_mul_f32 v[62:63], v[110:111], s[52:53]\n"
    "v_exp_f32 v56, v56\n"
    "v_exp_f32 v57, v57\n"
    "v_exp_f32 v58, v58\n"
    "v_exp_f32 v59, v59\n"
    "v_exp_f32 v60, v60\n"
    "v_exp_f32 v61, v61\n"
    "v_exp_f32 v62, v62\n"
    "v_exp_f32 v63, v63\n"
    "v_pk_add_f32 v[48:49], v[56:57], s[54:55]\n"
    "v_pk_add_f32 v[50:51], v[58:59], s[54:55]\n"
    "v_pk_add_f32 v[52:53], v[60:61], s[54:55]\n"
    "v_pk_add_f32 v[54:55], v[62:63], s[54:55]\n"
    "v_pk_fma_f32 v[56:57], v[56:57], v[56:57], v[48:49]\n"
    "v_pk_fma_f32 v[58:59], v[58:59], v[58:59], v[50:51]\n"
    "v_pk_fma_f32 v[60:61], v[60:61], v[60:61], v[52:53]\n"
    "v_pk_fma_f32 v[62:63], v[62:63], v[62:63], v[54:55]\n"
    "v_rcp_f32 v56, v56\n"
    "v_rcp_f32 v57, v57\n"
    "v_rcp_f32 v58, v58\n"
    "v_rcp_f32 v59, v59\n"
    "v_rcp_f32 v60, v60\n"
    "v_rcp_f32 v61, v61\n"
    "v_rcp_f32 v62, v62\n"
    "v_rcp_f32 v63, v63\n"
    "v_pk_mul_f32 v[48:49], v[48:49], v[56:57] clamp\n"
    "v_pk_mul_f32 v[50:51], v[50:51], v[58:59] clamp\n"
    "v_pk_mul_f32 v[52:53], v[52:53], v[60:61] clamp\n"
    "v_pk_mul_f32 v[54:55], v[54:55], v[62:63] clamp\n"
    "v_pk_mul_f32 v[48:49], v[100:101], v[48:49]\n"
    "v_pk_mul_f32 v[50:51], v[102:103], v[50:51]\n"
    "v_pk_mul_f32 v[52:53], v[108:109], v[52:53]\n"
    "v_pk_mul_f32 v[54:55], v[110:111], v[54:55]\n"
    "v_cvt_pk_bf16_f32 v100, v48, v49\n"
    "v_cvt_pk_bf16_f32 v101, v50, v51\n"
    "v_cvt_pk_bf16_f32 v102, v52, v53\n"
    "v_cvt_pk_bf16_f32 v103, v54, v55\n"
    "v_pk_mul_f32 v[56:57], v[112:113], s[52:53]\n"
    "v_pk_mul_f32 v[58:59], v[114:115], s[52:53]\n"
    "v_pk_mul_f32 v[60:61], v[120:121], s[52:53]\n"
    "v_pk_mul_f32 v[62:63], v[122:123], s[52:53]\n"
    "v_exp_f32 v56, v56\n"
    "v_exp_f32 v57, v57\n"
    "v_exp_f32 v58, v58\n"
    "v_exp_f32 v59, v59\n"
    "v_exp_f32 v60, v60\n"
    "v_exp_f32 v61, v61\n"
    "v_exp_f32 v62, v62\n"
    "v_exp_f32 v63, v63\n"
    "v_pk_add_f32 v[48:49], v[56:57], s[54:55]\n"
    "v_pk_add_f32 v[50:51], v[58:59], s[54:55]\n"
    "v_pk_add_f32 v[52:53], v[60:61], s[54:55]\n"
    "v_pk_add_f32 v[54:55], v[62:63], s[54:55]\n"
    "v_pk_fma_f32 v[56:57], v[56:57], v[56:57], v[48:49]\n"
    "v_pk_fma_f32 v[58:59], v[58:59], v[58:59], v[50:51]\n"
    "v_pk_fma_f32 v[60:61], v[60:61], v[60:61], v[52:53]\n"
    "v_pk_fma_f32 v[62:63], v[62:63], v[62:63], v[54:55]\n"
    "v_rcp_f32 v56, v56\n"
    "v_rcp_f32 v57, v57\n"
    "v_rcp_f32 v58, v58\n"
    "v_rcp_f32 v59, v59\n"
    "v_rcp_f32 v60, v60\n"
    "v_rcp_f32 v61, v61\n"
    "v_rcp_f32 v62, v62\n"
    "v_rcp_f32 v63, v63\n"
    "v_pk_mul_f32 v[48:49], v[48:49], v[56:57] clamp\n"
    "v_pk_mul_f32 v[50:51], v[50:51], v[58:59] clamp\n"
    "v_pk_mul_f32 v[52:53], v[52:53], v[60:61] clamp\n"
    "v_pk_mul_f32 v[54:55], v[54:55], v[62:63] clamp\n"
    "v_pk_mul_f32 v[48:49], v[112:113], v[48:49]\n"
    "v_pk_mul_f32 v[50:51], v[114:115], v[50:51]\n"
    "v_pk_mul_f32 v[52:53], v[120:121], v[52:53]\n"
    "v_pk_mul_f32 v[54:55], v[122:123], v[54:55]\n"
    "v_cvt_pk_bf16_f32 v112, v48, v49\n"
    "v_cvt_pk_bf16_f32 v113, v50, v51\n"
    "v_cvt_pk_bf16_f32 v114, v52, v53\n"
    "v_cvt_pk_bf16_f32 v115, v54, v55\n"
    "v_pk_mul_f32 v[56:57], v[116:117], s[52:53]\n"
    "v_pk_mul_f32 v[58:59], v[118:119], s[52:53]\n"
    "v_pk_mul_f32 v[60:61], v[124:125], s[52:53]\n"
    "v_pk_mul_f32 v[62:63], v[126:127], s[52:53]\n"
    "v_exp_f32 v56, v56\n"
    "v_exp_f32 v57, v57\n"
    "v_exp_f32 v58, v58\n"
    "v_exp_f32 v59, v59\n"
    "v_exp_f32 v60, v60\n"
    "v_exp_f32 v61, v61\n"
    "v_exp_f32 v62, v62\n"
    "v_exp_f32 v63, v63\n"
    "v_pk_add_f32 v[48:49], v[56:57], s[54:55]\n"
    "v_pk_add_f32 v[50:51], v[58:59], s[54:55]\n"
    "v_pk_add_f32 v[52:53], v[60:61], s[54:55]\n"
    "v_pk_add_f32 v[54:55], v[62:63], s[54:55]\n"
    "v_pk_fma_f32 v[56:57], v[56:57], v[56:57], v[48:49]\n"
    "v_pk_fma_f32 v[58:59], v[58:59], v[58:59], v[50:51]\n"
    "v_pk_fma_f32 v[60:61], v[60:61], v[60:61], v[52:53]\n"
    "v_pk_fma_f32 v[62:63], v[62:63], v[62:63], v[54:55]\n"
    "v_rcp_f32 v56, v56\n"
    "v_rcp_f32 v57, v57\n"
    "v_rcp_f32 v58, v58\n"
    "v_rcp_f32 v59, v59\n"
    "v_rcp_f32 v60, v60\n"
    "v_rcp_f32 v61, v61\n"
    "v_rcp_f32 v62, v62\n"
    "v_rcp_f32 v63, v63\n"
    "v_pk_mul_f32 v[48:49], v[48:49], v[56:57] clamp\n"
    "v_pk_mul_f32 v[50:51], v[50:51], v[58:59] clamp\n"
    "v_pk_mul_f32 v[52:53], v[52:53], v[60:61] clamp\n"
    "v_pk_mul_f32 v[54:55], v[54:55], v[62:63] clamp\n"
    "v_pk_mul_f32 v[48:49], v[116:117], v[48:49]\n"
    "v_pk_mul_f32 v[50:51], v[118:119], v[50:51]\n"
    "v_pk_mul_f32 v[52:53], v[124:125], v[52:53]\n"
    "v_pk_mul_f32 v[54:55], v[126:127], v[54:55]\n"
    "v_cvt_pk_bf16_f32 v116, v48, v49\n"
    "v_cvt_pk_bf16_f32 v117, v50, v51\n"
    "L_pad1:\n"
    "s_cbranch_execz L_back1\n"
    "s_nop 0\n"
    "v_cvt_pk_bf16_f32 v118, v52, v53\n"
    "v_cvt_pk_bf16_f32 v119, v54, v55\n"
    "buffer_store_dwordx4 v[96:99], v4, s[28:31], 0 offen offset:1024 sc1\n"
    "buffer_store_dwordx4 v[100:103], v4, s[28:31], s46 offen offset:1024 sc1\n"
    "buffer_store_dwordx4 v[112:115], v4, s[28:31], 0 offen offset:3072 sc1\n"
    "buffer_store_dwordx4 v[116:119], v4, s[28:31], s46 offen offset:3072 sc1\n"
    "L_p2b:\n"
    "s_barrier\n"
    "buffer_load_dwordx4 v[192:195], v20, s[20:23], s56 offen offset:0 sc1\n"
    "buffer_load_dwordx4 v[196:199], v21, s[20:23], s56 offen offset:0 sc1\n"
    "buffer_load_dwordx4 v[200:203], v22, s[20:23], s56 offen offset:0 sc1\n"
    "buffer_load_dwordx4 v[204:207], v23, s[20:23], s56 offen offset:0 sc1\n"
    "v_add_u32 v12, v6, v12\n"
    "v_add_u32 v13, v6, v13\n"
    "v_add_u32 v14, v6, v14\n"
    "v_add_u32 v15, v6, v15\n"
    "v_add_u32 v16, v6, v16\n"
    "v_add_u32 v17, v6, v17\n"
    "v_add_u32 v18, v6, v18\n"
    "v_add_u32 v19, v6, v19\n"
    "ds_read_b64_tr_b16 v[24:25], v12 offset:0\n"
    "ds_read_b64_tr_b16 v[26:27], v12 offset:2048\n"
    "ds_read_b64_tr_b16 v[28:29], v13 offset:0\n"
    "ds_read_b64_tr_b16 v[30:31], v13 offset:2048\n"
    "ds_read_b64_tr_b16 v[32:33], v14 offset:0\n"
    "ds_read_b64_tr_b16 v[34:35], v14 offset:2048\n"
    "ds_read_b64_tr_b16 v[36:37], v15 offset:0\n"
    "ds_read_b64_tr_b16 v[38:39], v15 offset:2048\n"
    "ds_read_b64_tr_b16 v[40:41], v12 offset:16384\n"
    "ds_read_b64_tr_b16 v[42:43], v12 offset:18432\n"
    "ds_read_b64_tr_b16 v[44:45], v13 offset:16384\n"
    "ds_read_b64_tr_b16 v[46:47], v13 offset:18432\n"
    "ds_read_b64_tr_b16 v[48:49], v14 offset:16384\n"
    "ds_read_b64_tr_b16 v[50:51], v14 offset:18432\n"
    "ds_read_b64_tr_b16 v[52:53], v15 offset:16384\n"
    "ds_read_b64_tr_b16 v[54:55], v15 offset:18432\n"
    "v_add_u32 v244, s8, v4\n"
    "v_add_u32 v245, s9, v4\n"
    "v_add_u32 v246, s10, v4\n"
    "v_add_u32 v247, s11, v4\n"
    "v_and_b32 v254, 7, v1\n"
    "v_lshlrev_b32 v254, 10, v254\n"
    "v_bfe_u32 v255, v1, 3, 1\n"
    "v_lshl_or_b32 v254, v255, 6, v254\n"
    "v_bfe_u32 v255, v1, 4, 1\n"
    "v_lshl_or_b32 v254, v255, 5, v254\n"
    "v_lshrrev_b32 v255, 5, v1\n"
    "v_lshl_or_b32 v248, v255, 4, v254\n"
    "s_waitcnt vmcnt(4)\n"
    "s_barrier\n"
    "buffer_load_dwordx4 v[208:211], v20, s[20:23], s57 offen offset:0 sc1\n"
    "buffer_load_dwordx4 v[212:215], v21, s[20:23], s57 offen offset:0 sc1\n"
    "buffer_load_dwordx4 v[216:219], v22, s[20:23], s57 offen offset:0 sc1\n"
    "buffer_load_dwordx4 v[220:223], v23, s[20:23], s57 offen offset:0 sc1\n"
    "buffer_load_dwordx4 v[224:227], v20, s[20:23], s58 offen offset:0 sc1\n"
    "L_p2c:\n"
    "s_waitcnt vmcnt(8) lgkmcnt(0)\n"
    "v_mfma_f32_16x16x32_bf16 v[64:67], v[128:131], v[192:195], 0\n"
    "v_mfma_f32_16x16x32_bf16 v[68:71], v[132:135], v[192:195], 0\n"
    "v_mfma_f32_16x16x32_bf16 v[72:75], v[136:139], v[192:195], 0\n"
    "buffer_load_dwordx4 v[228:231], v21, s[20:23], s58 offen offset:0 sc1\n"
    "v_mfma_f32_16x16x32_bf16 v[76:79], v[140:143], v[192:195], 0\n"
    "s_waitcnt vmcnt(8)\n"
    "v_mfma_f32_16x16x32_bf16 v[92:95], v[140:143], v[196:199], 0\n"
    "v_mfma_f32_16x16x32_bf16 v[88:91], v[136:139], v[196:199], 0\n"
    "v_mfma_f32_16x16x32_bf16 v[84:87], v[132:135], v[196:199], 0\n"
    "buffer_load_dwordx4 v[232:235], v22, s[20:23], s58 offen offset:0 sc1\n"
    "v_mfma_f32_16x16x32_bf16 v[80:83], v[128:131], v[196:199], 0\n"
    "s_waitcnt vmcnt(8)\n"
    "v_mfma_f32_16x16x32_bf16 v[96:99], v[128:131], v[200:203], 0\n"
    "v_mfma_f32_16x16x32_bf16 v[100:103], v[132:135], v[200:203], 0\n"
    "v_mfma_f32_16x16x32_bf16 v[104:107], v[136:139], v[200:203], 0\n"
    "buffer_load_dwordx4 v[192:195], v23, s[20:23], s58 offen offset:0 sc1\n"
    "v_mfma_f32_16x16x32_bf16 v[108:111], v[140:143], v[200:203], 0\n"
    "s_waitcnt vmcnt(8)\n"
    "v_mfma_f32_16x16x32_bf16 v[124:127], v[140:143], v[204:207], 0\n"
    "v_mfma_f32_16x16x32_bf16 v[120:123], v[136:139], v[204:207], 0\n"
    "v_mfma_f32_16x16x32_bf16 v[116:119], v[132:135], v[204:207], 0\n"
    "buffer_load_dwordx4 v[196:199], v20, s[20:23], s59 offen offset:0 sc1\n"
    "v_mfma_f32_16x16x32_bf16 v[112:115], v[128:131], v[204:207], 0\n"
    "s_waitcnt vmcnt(8)\n"
    "v_mfma_f32_16x16x32_bf16 v[64:67], v[144:147], v[208:211], v[64:67]\n"
    "v_mfma_f32_16x16x32_bf16 v[68:71], v[148:151], v[208:211], v[68:71]\n"
    "v_mfma_f32_16x16x32_bf16 v[72:75], v[152:155], v[208:211], v[72:75]\n"
    "buffer_load_dwordx4 v[200:203], v21, s[20:23], s59 offen offset:0 sc1\n"
    "v_mfma_f32_16x16x32_bf16 v[76:79], v[156:159], v[208:211], v[76:79]\n"
    "s_waitcnt vmcnt(8)\n"
    "v_mfma_f32_16x16x32_bf16 v[92:95], v[156:159], v[212:215], v[92:95]\n"
    "v_mfma_f32_16x16x32_bf16 v[88:91], v[152:155], v[212:215], v[88:91]\n"
    "v_mfma_f32_16x16x32_bf16 v[84:87], v[148:151], v[212:215], v[84:87]\n"
    "buffer_load_dwordx4 v[204:207], v22, s[20:23], s59 offen offset:0 sc1\n"
    "v_mfma_f32_16x16x32_bf16 v[80:83], v[144:147], v[212:215], v[80:83]\n"
    "s_waitcnt vmcnt(8)\n"
    "v_mfma_f32_16x16x32_bf16 v[96:99], v[144:147], v[216:219], v[96:99]\n"
    "v_mfma_f32_16x16x32_bf16 v[100:103], v[148:151], v[216:219], v[100:103]\n"
    "v_mfma_f32_16x16x32_bf16 v[104:107], v[152:155], v[216:219], v[104:107]\n"
    "buffer_load_dwordx4 v[208:211], v23, s[20:23], s59 offen offset:0 sc1\n"
    "v_mfma_f32_16x16x32_bf16 v[108:111], v[156:159], v[216:219], v[108:111]\n"
    "s_waitcnt vmcnt(8)\n"
    "v_mfma_f32_16x16x32_bf16 v[124:127], v[156:159], v[220:223], v[124:127]\n"
    "v_mfma_f32_16x16x32_bf16 v[120:123], v[152:155], v[220:223], v[120:123]\n"
    "v_mfma_f32_16x16x32_bf16 v[116:119], v[148:151], v[220:223], v[116:119]\n"
    "buffer_load_dwordx4 v[212:215], v20, s[20:23], s60 offen offset:0 sc1\n"
    "v_mfma_f32_16x16x32_bf16 v[112:115], v[144:147], v[220:223], v[112:115]\n"
    "s_waitcnt vmcnt(8)\n"
    "v_mfma_f32_16x16x32_bf16 v[64:67], v[160:163], v[224:227], v[64:67]\n"
    "v_mfma_f32_16x16x32_bf16 v[68:71], v[164:167], v[224:227], v[68:71]\n"
    "v_mfma_f32_16x16x32_bf16 v[72:75], v[168:171], v[224:227], v[72:75]\n"
    "buffer_load_dwordx4 v[216:219], v21, s[20:23], s60 offen offset:0 sc1\n"
    "v_mfma_f32_16x16x32_bf16 v[76:79], v[172:175], v[224:227], v[76:79]\n"
    "s_waitcnt vmcnt(8)\n"
    "v_mfma_f32_16x16x32_bf16 v[92:95], v[172:175], v[228:231], v[92:95]\n"
    "v_mfma_f32_16x16x32_bf16 v[88:91], v[168:171], v[228:231], v[88:91]\n"
    "v_mfma_f32_16x16x32_bf16 v[84:87], v[164:167], v[228:231], v[84:87]\n"
    "buffer_load_dwordx4 v[220:223], v22, s[20:23], s60 offen offset:0 sc1\n"
    "v_mfma_f32_16x16x32_bf16 v[80:83], v[160:163], v[228:231], v[80:83]\n"
    "s_waitcnt vmcnt(8)\n"
    "v_mfma_f32_16x16x32_bf16 v[96:99], v[160:163], v[232:235], v[96:99]\n"
    "v_mfma_f32_16x16x32_bf16 v[100:103], v[164:167], v[232:235], v[100:103]\n"
    "v_mfma_f32_16x16x32_bf16 v[104:107], v[168:171], v[232:235], v[104:107]\n"
    "buffer_load_dwordx4 v[224:227], v23, s[20:23], s60 offen offset:0 sc1\n"
    "v_mfma_f32_16x16x32_bf16 v[108:111], v[172:175], v[232:235], v[108:111]\n"
    "s_waitcnt vmcnt(8)\n"
    "v_mfma_f32_16x16x32_bf16 v[124:127], v[172:175], v[192:195], v[124:127]\n"
    "v_mfma_f32_16x16x32_bf16 v[120:123], v[168:171], v[192:195], v[120:123]\n"
    "v_mfma_f32_16x16x32_bf16 v[116:119], v[164:167], v[192:195], v[116:119]\n"
    "buffer_load_dwordx4 v[228:231], v20, s[20:23], s61 offen offset:0 sc1\n"
    "v_mfma_f32_16x16x32_bf16 v[112:115], v[160:163], v[192:195], v[112:115]\n"
    "s_waitcnt vmcnt(8)\n"
    "v_mfma_f32_16x16x32_bf16 v[64:67], v[176:179], v[196:199], v[64:67]\n"
    "v_mfma_f32_16x16x32_bf16 v[68:71], v[180:183], v[196:199], v[68:71]\n"
    "v_mfma_f32_16x16x32_bf16 v[72:75], v[184:187], v[196:199], v[72:75]\n"
    "buffer_load_dwordx4 v[232:235], v21, s[20:23], s61 offen offset:0 sc1\n"
    "v_mfma_f32_16x16x32_bf16 v[76:79], v[188:191], v[196:199], v[76:79]\n"
    "s_waitcnt vmcnt(8)\n"
    "v_mfma_f32_16x16x32_bf16 v[92:95], v[188:191], v[200:203], v[92:95]\n"
    "v_mfma_f32_16x16x32_bf16 v[88:91], v[184:187], v[200:203], v[88:91]\n"
    "v_mfma_f32_16x16x32_bf16 v[84:87], v[180:183], v[200:203], v[84:87]\n"
    "buffer_load_dwordx4 v[192:195], v22, s[20:23], s61 offen offset:0 sc1\n"
    "v_mfma_f32_16x16x32_bf16 v[80:83], v[176:179], v[200:203], v[80:83]\n"
    "s_waitcnt vmcnt(8)\n"
    "v_mfma_f32_16x16x32_bf16 v[96:99], v[176:179], v[204:207], v[96:99]\n"
    "v_mfma_f32_16x16x32_bf16 v[100:103], v[180:183], v[204:207], v[100:103]\n"
    "v_mfma_f32_16x16x32_bf16 v[104:107], v[184:187], v[204:207], v[104:107]\n"
    "buffer_load_dwordx4 v[196:199], v23, s[20:23], s61 offen offset:0 sc1\n"
    "v_mfma_f32_16x16x32_bf16 v[108:111], v[188:191], v[204:207], v[108:111]\n"
    "s_waitcnt vmcnt(8)\n"
    "v_mfma_f32_16x16x32_bf16 v[124:127], v[188:191], v[208:211], v[124:127]\n"
    "v_mfma_f32_16x16x32_bf16 v[120:123], v[184:187], v[208:211], v[120:123]\n"
    "v_mfma_f32_16x16x32_bf16 v[116:119], v[180:183], v[208:211], v[116:119]\n"
    "buffer_load_dwordx4 v[200:203], v20, s[20:23], s62 offen offset:0 sc1\n"
    "v_mfma_f32_16x16x32_bf16 v[112:115], v[176:179], v[208:211], v[112:115]\n"
    "s_waitcnt vmcnt(8)\n"
    "v_mfma_f32_16x16x32_bf16 v[64:67], v[24:27], v[212:215], v[64:67]\n"
    "v_mfma_f32_16x16x32_bf16 v[68:71], v[28:31], v[212:215], v[68:71]\n"
    "v_mfma_f32_16x16x32_bf16 v[72:75], v[32:35], v[212:215], v[72:75]\n"
    "buffer_load_dwordx4 v[204:207], v21, s[20:23], s62 offen offset:0 sc1\n"
    "v_mfma_f32_16x16x32_bf16 v[76:79], v[36:39], v[212:215], v[76:79]\n"
    "s_waitcnt vmcnt(8)\n"
    "v_mfma_f32_16x16x32_bf16 v[92:95], v[36:39], v[216:219], v[92:95]\n"
    "v_mfma_f32_16x16x32_bf16 v[88:91], v[32:35], v[216:219], v[88:91]\n"
    "v_mfma_f32_16x16x32_bf16 v[84:87], v[28:31], v[216:219], v[84:87]\n"
    "buffer_load_dwordx4 v[208:211], v22, s[20:23], s62 offen offset:0 sc1\n"
    "v_mfma_f32_16x16x32_bf16 v[80:83], v[24:27], v[216:219], v[80:83]\n"
    "s_waitcnt vmcnt(8)\n"
    "v_mfma_f32_16x16x32_bf16 v[96:99], v[24:27], v[220:223], v[96:99]\n"
    "v_mfma_f32_16x16x32_bf16 v[100:103], v[28:31], v[220:223], v[100:103]\n"
    "v_mfma_f32_16x16x32_bf16 v[104:107], v[32:35], v[220:223], v[104:107]\n"
    "buffer_load_dwordx4 v[212:215], v23, s[20:23], s62 offen offset:0 sc1\n"
    "v_mfma_f32_16x16x32_bf16 v[108:111], v[36:39], v[220:223], v[108:111]\n"
    "s_waitcnt vmcnt(8)\n"
    "v_mfma_f32_16x16x32_bf16 v[124:127], v[36:39], v[224:227], v[124:127]\n"
    "v_mfma_f32_16x16x32_bf16 v[120:123], v[32:35], v[224:227], v[120:123]\n"
    "v_mfma_f32_16x16x32_bf16 v[116:119], v[28:31], v[224:227], v[116:119]\n"
    "v_mfma_f32_16x16x32_bf16 v[112:115], v[24:27], v[224:227], v[112:115]\n"
    "s_waitcnt vmcnt(7)\n"
    "s_barrier\n"
    "buffer_load_dwordx4 v[216:219], v20, s[20:23], s56 offen offset:1024 sc1\n"
    "v_mfma_f32_16x16x32_bf16 v[64:67], v[40:43], v[228:231], v[64:67]\n"
    "ds_read_b64_tr_b16 v[24:25], v12 offset:32768\n"
    "v_mfma_f32_16x16x32_bf16 v[68:71], v[44:47], v[228:231], v[68:71]\n"
    "ds_read_b64_tr_b16 v[26:27], v12 offset:34816\n"
    "v_mfma_f32_16x16x32_bf16 v[72:75], v[48:51], v[228:231], v[72:75]\n"
    "buffer_load_dwordx4 v[220:223], v21, s[20:23], s56 offen offset:1024 sc1\n"
    "v_mfma_f32_16x16x32_bf16 v[76:79], v[52:55], v[228:231], v[76:79]\n"
    "ds_read_b64_tr_b16 v[28:29], v13 offset:32768\n"
    "s_waitcnt vmcnt(8)\n"
    "v_mfma_f32_16x16x32_bf16 v[92:95], v[52:55], v[232:235], v[92:95]\n"
    "ds_read_b64_tr_b16 v[30:31], v13 offset:34816\n"
    "v_mfma_f32_16x16x32_bf16 v[88:91], v[48:51], v[232:235], v[88:91]\n"
    "ds_read_b64_tr_b16 v[32:33], v14 offset:32768\n"
    "v_mfma_f32_16x16x32_bf16 v[84:87], v[44:47], v[232:235], v[84:87]\n"
    "buffer_load_dwordx4 v[224:227], v22, s[20:23], s56 offen offset:1024 sc1\n"
    "v_mfma_f32_16x16x32_bf16 v[80:83], v[40:43], v[232:235], v[80:83]\n"
    "ds_read_b64_tr_b16 v[34:35], v14 offset:34816\n"
    "s_waitcnt vmcnt(8)\n"
    "v_mfma_f32_16x16x32_bf16 v[96:99], v[40:43], v[192:195], v[96:99]\n"
    "ds_read_b64_tr_b16 v[36:37], v15 offset:32768\n"
    "v_mfma_f32_16x16x32_bf16 v[100:103], v[44:47], v[192:195], v[100:103]\n"
    "ds_read_b64_tr_b16 v[38:39], v15 offset:34816\n"
    "v_mfma_f32_16x16x32_bf16 v[104:107], v[48:51], v[192:195], v[104:107]\n"
    "buffer_load_dwordx4 v[228:231], v23, s[20:23], s56 offen offset:1024 sc1\n"
    "v_mfma_f32_16x16x32_bf16 v[108:111], v[52:55], v[192:195], v[108:111]\n"
    "ds_read_b64_tr_b16 v[128:129], v8 offset:16384\n"
    "s_waitcnt vmcnt(8)\n"
    "v_mfma_f32_16x16x32_bf16 v[124:127], v[52:55], v[196:199], v[124:127]\n"
    "ds_read_b64_tr_b16 v[130:131], v8 offset:18432\n"
    "v_mfma_f32_16x16x32_bf16 v[120:123], v[48:51], v[196:199], v[120:123]\n"
    "ds_read_b64_tr_b16 v[132:133], v9 offset:16384\n"
    "v_mfma_f32_16x16x32_bf16 v[116:119], v[44:47], v[196:199], v[116:119]\n"
    "buffer_load_dwordx4 v[232:235], v20, s[20:23], s57 offen offset:1024 sc1\n"
    "v_mfma_f32_16x16x32_bf16 v[112:115], v[40:43], v[196:199], v[112:115]\n"
    "ds_read_b64_tr_b16 v[134:135], v9 offset:18432\n"
    "s_waitcnt vmcnt(8) lgkmcnt(4)\n"
    "v_mfma_f32_16x16x32_bf16 v[64:67], v[24:27], v[200:203], v[64:67]\n"
    "ds_read_b64_tr_b16 v[40:41], v12 offset:49152\n"
    "v_mfma_f32_16x16x32_bf16 v[68:71], v[28:31], v[200:203], v[68:71]\n"
    "ds_read_b64_tr_b16 v[42:43], v12 offset:51200\n"
    "v_mfma_f32_16x16x32_bf16 v[72:75], v[32:35], v[200:203], v[72:75]\n"
    "buffer_load_dwordx4 v[192:195], v21, s[20:23], s57 offen offset:1024 sc1\n"
    "v_mfma_f32_16x16x32_bf16 v[76:79], v[36:39], v[200:203], v[76:79]\n"
    "ds_read_b64_tr_b16 v[44:45], v13 offset:49152\n"
    "s_waitcnt vmcnt(8)\n"
    "v_mfma_f32_16x16x32_bf16 v[92:95], v[36:39], v[204:207], v[92:95]\n"
    "ds_read_b64_tr_b16 v[46:47], v13 offset:51200\n"
    "v_mfma_f32_16x16x32_bf16 v[88:91], v[32:35], v[204:207], v[88:91]\n"
    "ds_read_b64_tr_b16 v[48:49], v14 offset:49152\n"
    "v_mfma_f32_16x16x32_bf16 v[84:87], v[28:31], v[204:207], v[84:87]\n"
    "buffer_load_dwordx4 v[196:199], v22, s[20:23], s57 offen offset:1024 sc1\n"
    "v_mfma_f32_16x16x32_bf16 v[80:83], v[24:27], v[204:207], v[80:83]\n"
    "ds_read_b64_tr_b16 v[50:51], v14 offset:51200\n"
    "s_waitcnt vmcnt(8)\n"
    "v_mfma_f32_16x16x32_bf16 v[96:99], v[24:27], v[208:211], v[96:99]\n"
    "ds_read_b64_tr_b16 v[52:53], v15 offset:49152\n"
    "v_mfma_f32_16x16x32_bf16 v[100:103], v[28:31], v[208:211], v[100:103]\n"
    "ds_read_b64_tr_b16 v[54:55], v15 offset:51200\n"
    "v_mfma_f32_16x16x32_bf16 v[104:107], v[32:35], v[208:211], v[104:107]\n"
    "buffer_load_dwordx4 v[200:203], v23, s[20:23], s57 offen offset:1024 sc1\n"
    "v_mfma_f32_16x16x32_bf16 v[108:111], v[36:39], v[208:211], v[108:111]\n"
    "ds_read_b64_tr_b16 v[136:137], v10 offset:16384\n"
    "s_waitcnt vmcnt(8)\n"
    "v_mfma_f32_16x16x32_bf16 v[124:127], v[36:39], v[212:215], v[124:127]\n"
    "ds_read_b64_tr_b16 v[138:139], v10 offset:18432\n"
    "v_mfma_f32_16x16x32_bf16 v[120:123], v[32:35], v[212:215], v[120:123]\n"
    "ds_read_b64_tr_b16 v[140:141], v11 offset:16384\n"
    "v_mfma_f32_16x16x32_bf16 v[116:119], v[28:31], v[212:215], v[116:119]\n"
    "buffer_load_dwordx4 v[204:207], v20, s[20:23], s58 offen offset:1024 sc1\n"
    "v_mfma_f32_16x16x32_bf16 v[112:115], v[24:27], v[212:215], v[112:115]\n"
    "ds_read_b64_tr_b16 v[142:143], v11 offset:18432\n"
    "s_waitcnt vmcnt(8) lgkmcnt(4)\n"
    "v_mfma_f32_16x16x32_bf16 v[64:67], v[40:43], v[216:219], v[64:67]\n"
    "ds_read_b64_tr_b16 v[24:25], v16 offset:0\n"
    "v_mfma_f32_16x16x32_bf16 v[68:71], v[44:47], v[216:219], v[68:71]\n"
    "ds_read_b64_tr_b16 v[26:27], v16 offset:2048\n"
    "v_mfma_f32_16x16x32_bf16 v[72:75], v[48:51], v[216:219], v[72:75]\n"
    "buffer_load_dwordx4 v[208:211], v21, s[20:23], s58 offen offset:1024 sc1\n"
    "v_mfma_f32_16x16x32_bf16 v[76:79], v[52:55], v[216:219], v[76:79]\n"
    "ds_read_b64_tr_b16 v[28:29], v17 offset:0\n"
    "s_waitcnt vmcnt(8)\n"
    "v_mfma_f32_16x16x32_bf16 v[92:95], v[52:55], v[220:223], v[92:95]\n"
    "ds_read_b64_tr_b16 v[30:31], v17 offset:2048\n"
    "v_mfma_f32_16x16x32_bf16 v[88:91], v[48:51], v[220:223], v[88:91]\n"
    "ds_read_b64_tr_b16 v[32:33], v18 offset:0\n"
    "v_mfma_f32_16x16x32_bf16 v[84:87], v[44:47], v[220:223], v[84:87]\n"
    "buffer_load_dwordx4 v[212:215], v22, s[20:23], s58 offen offset:1024 sc1\n"
    "v_mfma_f32_16x16x32_bf16 v[80:83], v[40:43], v[220:223], v[80:83]\n"
    "ds_read_b64_tr_b16 v[34:35], v18 offset:2048\n"
    "s_waitcnt vmcnt(8)\n"
    "v_mfma_f32_16x16x32_bf16 v[96:99], v[40:43], v[224:227], v[96:99]\n"
    "ds_read_b64_tr_b16 v[36:37], v19 offset:0\n"
    "v_mfma_f32_16x16x32_bf16 v[100:103], v[44:47], v[224:227], v[100:103]\n"
    "ds_read_b64_tr_b16 v[38:39], v19 offset:2048\n"
    "v_mfma_f32_16x16x32_bf16 v[104:107], v[48:51], v[224:227], v[104:107]\n"
    "buffer_load_dwordx4 v[216:219], v23, s[20:23], s58 offen offset:1024 sc1\n"
    "v_mfma_f32_16x16x32_bf16 v[108:111], v[52:55], v[224:227], v[108:111]\n"
    "ds_read_b64_tr_b16 v[144:145], v8 offset:32768\n"
    "s_waitcnt vmcnt(8)\n"
    "v_mfma_f32_16x16x32_bf16 v[124:127], v[52:55], v[228:231], v[124:127]\n"
    "ds_read_b64_tr_b16 v[146:147], v8 offset:34816\n"
    "v_mfma_f32_16x16x32_bf16 v[120:123], v[48:51], v[228:231], v[120:123]\n"
    "ds_read_b64_tr_b16 v[148:149], v9 offset:32768\n"
    "v_mfma_f32_16x16x32_bf16 v[116:119], v[44:47], v[228:231], v[116:119]\n"
    "buffer_load_dwordx4 v[220:223], v20, s[20:23], s59 offen offset:1024 sc1\n"
    "v_mfma_f32_16x16x32_bf16 v[112:115], v[40:43], v[228:231], v[112:115]\n"
    "ds_read_b64_tr_b16 v[150:151], v9 offset:34816\n"
    "s_waitcnt vmcnt(8) lgkmcnt(4)\n"
    "v_mfma_f32_16x16x32_bf16 v[64:67], v[24:27], v[232:235], v[64:67]\n"
    "ds_read_b64_tr_b16 v[40:41], v16 offset:16384\n"
    "v_mfma_f32_16x16x32_bf16 v[68:71], v[28:31], v[232:235], v[68:71]\n"
    "ds_read_b64_tr_b16 v[42:43], v16 offset:18432\n"
    "v_mfma_f32_16x16x32_bf16 v[72:75], v[32:35], v[232:235], v[72:75]\n"
    "buffer_load_dwordx4 v[224:227], v20, s[20:23], s60 offen offset:1024 sc1\n"
    "v_mfma_f32_16x16x32_bf16 v[76:79], v[36:39], v[232:235], v[76:79]\n"
    "ds_read_b64_tr_b16 v[44:45], v17 offset:16384\n"
    "s_waitcnt vmcnt(8)\n"
    "v_mfma_f32_16x16x32_bf16 v[92:95], v[36:39], v[192:195], v[92:95]\n"
    "ds_read_b64_tr_b16 v[46:47], v17 offset:18432\n"
    "v_mfma_f32_16x16x32_bf16 v[88:91], v[32:35], v[192:195], v[88:91]\n"
    "ds_read_b64_tr_b16 v[48:49], v18 offset:16384\n"
    "v_mfma_f32_16x16x32_bf16 v[84:87], v[28:31], v[192:195], v[84:87]\n"
    "buffer_load_dwordx4 v[228:231], v20, s[20:23], s61 offen offset:1024 sc1\n"
    "v_mfma_f32_16x16x32_bf16 v[80:83], v[24:27], v[192:195], v[80:83]\n"
    "ds_read_b64_tr_b16 v[50:51], v18 offset:18432\n"
    "s_waitcnt vmcnt(8)\n"
    "v_mfma_f32_16x16x32_bf16 v[96:99], v[24:27], v[196:199], v[96:99]\n"
    "ds_read_b64_tr_b16 v[52:53], v19 offset:16384\n"
    "v_mfma_f32_16x16x32_bf16 v[100:103], v[28:31], v[196:199], v[100:103]\n"
    "ds_read_b64_tr_b16 v[54:55], v19 offset:18432\n"
    "v_mfma_f32_16x16x32_bf16 v[104:107], v[32:35], v[196:199], v[104:107]\n"
    "buffer_load_dwordx4 v[232:235], v20, s[20:23], s62 offen offset:1024 sc1\n"
    "v_mfma_f32_16x16x32_bf16 v[108:111], v[36:39], v[196:199], v[108:111]\n"
    "ds_read_b64_tr_b16 v[152:153], v10 offset:32768\n"
    "s_waitcnt vmcnt(8)\n"
    "v_mfma_f32_16x16x32_bf16 v[124:127], v[36:39], v[200:203], v[124:127]\n"
    "ds_read_b64_tr_b16 v[154:155], v10 offset:34816\n"
    "v_mfma_f32_16x16x32_bf16 v[120:123], v[32:35], v[200:203], v[120:123]\n"
    "ds_read_b64_tr_b16 v[156:157], v11 offset:32768\n"
    "v_mfma_f32_16x16x32_bf16 v[116:119], v[28:31], v[200:203], v[116:119]\n"
    "buffer_load_dwordx4 v[192:195], v21, s[20:23], s59 offen offset:1024 sc1\n"
    "v_mfma_f32_16x16x32_bf16 v[112:115], v[24:27], v[200:203], v[112:115]\n"
    "ds_read_b64_tr_b16 v[158:159], v11 offset:34816\n"
    "s_waitcnt vmcnt(8) lgkmcnt(4)\n"
    "v_mfma_f32_16x16x32_bf16 v[64:67], v[40:43], v[204:207], v[64:67]\n"
    "ds_read_b64_tr_b16 v[24:25], v8 offset:0\n"
    "v_mfma_f32_16x16x32_bf16 v[68:71], v[44:47], v[204:207], v[68:71]\n"
    "ds_read_b64_tr_b16 v[26:27], v8 offset:2048\n"
    "v_mfma_f32_16x16x32_bf16 v[72:75], v[48:51], v[204:207], v[72:75]\n"
    "buffer_load_dwordx4 v[196:199], v21, s[20:23], s60 offen offset:1024 sc1\n"
    "v_mfma_f32_16x16x32_bf16 v[76:79], v[52:55], v[204:207], v[76:79]\n"
    "ds_read_b64_tr_b16 v[28:29], v9 offset:0\n"
    "s_waitcnt vmcnt(8)\n"
    "v_mfma_f32_16x16x32_bf16 v[92:95], v[52:55], v[208:211], v[92:95]\n"
    "ds_read_b64_tr_b16 v[30:31], v9 offset:2048\n"
    "v_mfma_f32_16x16x32_bf16 v[88:91], v[48:51], v[208:211], v[88:91]\n"
    "ds_read_b64_tr_b16 v[32:33], v10 offset:0\n"
    "v_mfma_f32_16x16x32_bf16 v[84:87], v[44:47], v[208:211], v[84:87]\n"
    "buffer_load_dwordx4 v[200:203], v21, s[20:23], s61 offen offset:1024 sc1\n"
    "v_mfma_f32_16x16x32_bf16 v[80:83], v[40:43], v[208:211], v[80:83]\n"
    "ds_read_b64_tr_b16 v[34:35], v10 offset:2048\n"
    "s_waitcnt vmcnt(8)\n"
    "v_mfma_f32_16x16x32_bf16 v[96:99], v[40:43], v[212:215], v[96:99]\n"
    "ds_read_b64_tr_b16 v[36:37], v11 offset:0\n"
    "v_mfma_f32_16x16x32_bf16 v[100:103], v[44:47], v[212:215], v[100:103]\n"
    "ds_read_b64_tr_b16 v[38:39], v11 offset:2048\n"
    "v_mfma_f32_16x16x32_bf16 v[104:107], v[48:51], v[212:215], v[104:107]\n"
    "buffer_load_dwordx4 v[204:207], v21, s[20:23], s62 offen offset:1024 sc1\n"
    "v_mfma_f32_16x16x32_bf16 v[108:111], v[52:55], v[212:215], v[108:111]\n"
    "ds_read_b64_tr_b16 v[160:161], v8 offset:49152\n"
    "s_waitcnt vmcnt(8)\n"
    "v_mfma_f32_16x16x32_bf16 v[124:127], v[52:55], v[216:219], v[124:127]\n"
    "ds_read_b64_tr_b16 v[162:163], v8 offset:51200\n"
    "v_mfma_f32_16x16x32_bf16 v[120:123], v[48:51], v[216:219], v[120:123]\n"
    "ds_read_b64_tr_b16 v[164:165], v9 offset:49152\n"
    "v_mfma_f32_16x16x32_bf16 v[116:119], v[44:47], v[216:219], v[116:119]\n"
    "buffer_load_dwordx4 v[208:211], v22, s[20:23], s59 offen offset:1024 sc1\n"
    "v_mfma_f32_16x16x32_bf16 v[112:115], v[40:43], v[216:219], v[112:115]\n"
    "ds_read_b64_tr_b16 v[166:167], v9 offset:51200\n"
    "s_waitcnt vmcnt(8) lgkmcnt(4)\n"
    "v_mfma_f32_16x16x32_bf16 v[64:67], v[24:27], v[220:223], v[64:67]\n"
    "ds_read_b64_tr_b16 v[168:169], v10 offset:49152\n"
    "v_mfma_f32_16x16x32_bf16 v[68:71], v[28:31], v[220:223], v[68:71]\n"
    "ds_read_b64_tr_b16 v[170:171], v10 offset:51200\n"
    "v_mfma_f32_16x16x32_bf16 v[72:75], v[32:35], v[220:223], v[72:75]\n"
    "buffer_load_dwordx4 v[212:215], v22, s[20:23], s60 offen offset:1024 sc1\n"
    "v_mfma_f32_16x16x32_bf16 v[76:79], v[36:39], v[220:223], v[76:79]\n"
    "ds_read_b64_tr_b16 v[172:173], v11 offset:49152\n"
    "s_waitcnt vmcnt(8) lgkmcnt(15)\n"
    "v_mfma_f32_16x16x32_bf16 v[76:79], v[140:143], v[224:227], v[76:79]\n"
    "ds_read_b64_tr_b16 v[174:175], v11 offset:51200\n"
    "v_mfma_f32_16x16x32_bf16 v[72:75], v[136:139], v[224:227], v[72:75]\n"
    "v_mfma_f32_16x16x32_bf16 v[68:71], v[132:135], v[224:227], v[68:71]\n"
    "buffer_load_dwordx4 v[216:219], v22, s[20:23], s61 offen offset:1024 sc1\n"
    "v_mfma_f32_16x16x32_bf16 v[64:67], v[128:131], v[224:227], v[64:67]\n"
    "s_waitcnt vmcnt(8) lgkmcnt(15)\n"
    "v_mfma_f32_16x16x32_bf16 v[64:67], v[144:147], v[228:231], v[64:67]\n"
    "v_mfma_f32_16x16x32_bf16 v[68:71], v[148:151], v[228:231], v[68:71]\n"
    "v_mfma_f32_16x16x32_bf16 v[72:75], v[152:155], v[228:231], v[72:75]\n"
    "buffer_load_dwordx4 v[220:223], v22, s[20:23], s62 offen offset:1024 sc1\n"
    "v_mfma_f32_16x16x32_bf16 v[76:79], v[156:159], v[228:231], v[76:79]\n"
    "s_waitcnt vmcnt(8) lgkmcnt(0)\n"
    "v_mfma_f32_16x16x32_bf16 v[76:79], v[172:175], v[232:235], v[76:79]\n"
    "v_mfma_f32_16x16x32_bf16 v[72:75], v[168:171], v[232:235], v[72:75]\n"
    "v_mfma_f32_16x16x32_bf16 v[68:71], v[164:167], v[232:235], v[68:71]\n"
    "buffer_load_dwordx4 v[224:227], v23, s[20:23], s59 offen offset:1024 sc1\n"
    "v_mfma_f32_16x16x32_bf16 v[64:67], v[160:163], v[232:235], v[64:67]\n"
    "s_waitcnt vmcnt(8)\n"
    "v_mfma_f32_16x16x32_bf16 v[80:83], v[24:27], v[192:195], v[80:83]\n"
    "v_mfma_f32_16x16x32_bf16 v[84:87], v[28:31], v[192:195], v[84:87]\n"
    "ds_write_b128 v247, v[76:79] offset:3072\n"
    "v_mfma_f32_16x16x32_bf16 v[88:91], v[32:35], v[192:195], v[88:91]\n"
    "buffer_load_dwordx4 v[228:231], v23, s[20:23], s60 offen offset:1024 sc1\n"
    "v_mfma_f32_16x16x32_bf16 v[92:95], v[36:39], v[192:195], v[92:95]\n"
    "s_waitcnt vmcnt(8)\n"
    "v_mfma_f32_16x16x32_bf16 v[92:95], v[140:143], v[196:199], v[92:95]\n"
    "v_mfma_f32_16x16x32_bf16 v[88:91], v[136:139], v[196:199], v[88:91]\n"
    "ds_write_b128 v247, v[72:75] offset:2048\n"
    "v_mfma_f32_16x16x32_bf16 v[84:87], v[132:135], v[196:199], v[84:87]\n"
    "buffer_load_dwordx4 v[232:235], v23, s[20:23], s61 offen offset:1024 sc1\n"
    "v_mfma_f32_16x16x32_bf16 v[80:83], v[128:131], v[196:199], v[80:83]\n"
    "s_waitcnt vmcnt(8)\n"
    "v_mfma_f32_16x16x32_bf16 v[80:83], v[144:147], v[200:203], v[80:83]\n"
    "v_mfma_f32_16x16x32_bf16 v[84:87], v[148:151], v[200:203], v[84:87]\n"
    "ds_write_b128 v247, v[68:71] offset:1024\n"
    "v_mfma_f32_16x16x32_bf16 v[88:91], v[152:155], v[200:203], v[88:91]\n"
    "buffer_load_dwordx4 v[192:195], v23, s[20:23], s62 offen offset:1024 sc1\n"
    "v_mfma_f32_16x16x32_bf16 v[92:95], v[156:159], v[200:203], v[92:95]\n"
    "s_waitcnt vmcnt(8)\n"
    "v_mfma_f32_16x16x32_bf16 v[92:95], v[172:175], v[204:207], v[92:95]\n"
    "v_mfma_f32_16x16x32_bf16 v[88:91], v[168:171], v[204:207], v[88:91]\n"
    "ds_write_b128 v247, v[64:67] offset:0\n"
    "v_mfma_f32_16x16x32_bf16 v[84:87], v[164:167], v[204:207], v[84:87]\n"
    "v_mfma_f32_16x16x32_bf16 v[80:83], v[160:163], v[204:207], v[80:83]\n"
    "s_waitcnt vmcnt(7)\n"
    "v_mfma_f32_16x16x32_bf16 v[96:99], v[24:27], v[208:211], v[96:99]\n"
    "v_mfma_f32_16x16x32_bf16 v[100:103], v[28:31], v[208:211], v[100:103]\n"
    "ds_write_b128 v247, v[92:95] offset:19456\n"
    "v_mfma_f32_16x16x32_bf16 v[104:107], v[32:35], v[208:211], v[104:107]\n"
    "v_mfma_f32_16x16x32_bf16 v[108:111], v[36:39], v[208:211], v[108:111]\n"
    "s_waitcnt vmcnt(6)\n"
    "v_mfma_f32_16x16x32_bf16 v[108:111], v[140:143], v[212:215], v[108:111]\n"
    "v_mfma_f32_16x16x32_bf16 v[104:107], v[136:139], v[212:215], v[104:107]\n"
    "ds_write_b128 v247, v[88:91] offset:18432\n"
    "v_mfma_f32_16x16x32_bf16 v[100:103], v[132:135], v[212:215], v[100:103]\n"
    "v_mfma_f32_16x16x32_bf16 v[96:99], v[128:131], v[212:215], v[96:99]\n"
    "s_waitcnt vmcnt(5)\n"
    "v_mfma_f32_16x16x32_bf16 v[96:99], v[144:147], v[216:219], v[96:99]\n"
    "v_mfma_f32_16x16x32_bf16 v[100:103], v[148:151], v[216:219], v[100:103]\n"
    "ds_write_b128 v247, v[84:87] offset:17408\n"
    "v_mfma_f32_16x16x32_bf16 v[104:107], v[152:155], v[216:219], v[104:107]\n"
    "v_mfma_f32_16x16x32_bf16 v[108:111], v[156:159], v[216:219], v[108:111]\n"
    "s_waitcnt vmcnt(4)\n"
    "v_mfma_f32_16x16x32_bf16 v[108:111], v[172:175], v[220:223], v[108:111]\n"
    "v_mfma_f32_16x16x32_bf16 v[104:107], v[168:171], v[220:223], v[104:107]\n"
    "ds_write_b128 v247, v[80:83] offset:16384\n"
    "v_mfma_f32_16x16x32_bf16 v[100:103], v[164:167], v[220:223], v[100:103]\n"
    "v_mfma_f32_16x16x32_bf16 v[96:99], v[160:163], v[220:223], v[96:99]\n"
    "s_waitcnt vmcnt(3)\n"
    "v_mfma_f32_16x16x32_bf16 v[124:127], v[36:39], v[224:227], v[124:127]\n"
    "v_mfma_f32_16x16x32_bf16 v[120:123], v[32:35], v[224:227], v[120:123]\n"
    "ds_write_b128 v247, v[108:111] offset:35840\n"
    "v_mfma_f32_16x16x32_bf16 v[116:119], v[28:31], v[224:227], v[116:119]\n"
    "v_mfma_f32_16x16x32_bf16 v[112:115], v[24:27], v[224:227], v[112:115]\n"
    "ds_write_b128 v247, v[104:107] offset:34816\n"
    "s_waitcnt vmcnt(2)\n"
    "s_waitcnt lgkmcnt(2)\n"
    "s_barrier\n"
    "v_mfma_f32_16x16x32_bf16 v[112:115], v[128:131], v[228:231], v[112:115]\n"
    "ds_read_b128 v[64:67], v244 offset:0\n"
    "v_mfma_f32_16x16x32_bf16 v[116:119], v[132:135], v[228:231], v[116:119]\n"
    "ds_write_b128 v247, v[100:103] offset:33792\n"
    "v_mfma_f32_16x16x32_bf16 v[120:123], v[136:139], v[228:231], v[120:123]\n"
    "ds_read_b128 v[68:71], v244 offset:1024\n"
    "v_mfma_f32_16x16x32_bf16 v[124:127], v[140:143], v[228:231], v[124:127]\n"
    "ds_write_b128 v247, v[96:99] offset:32768\n"
    "s_waitcnt vmcnt(1)\n"
    "v_mfma_f32_16x16x32_bf16 v[124:127], v[156:159], v[232:235], v[124:127]\n"
    "ds_read_b128 v[72:75], v244 offset:2048\n"
    "v_mfma_f32_16x16x32_bf16 v[120:123], v[152:155], v[232:235], v[120:123]\n"
    "ds_read_b128 v[76:79], v244 offset:3072\n"
    "s_waitcnt lgkmcnt(2)\n"
    "s_barrier\n"
    "v_mfma_f32_16x16x32_bf16 v[116:119], v[148:151], v[232:235], v[116:119]\n"
    "ds_read_b128 v[80:83], v245 offset:0\n"
    "v_mfma_f32_16x16x32_bf16 v[112:115], v[144:147], v[232:235], v[112:115]\n"
    "ds_read_b128 v[84:87], v245 offset:1024\n"
    "s_waitcnt vmcnt(0)\n"
    "v_mfma_f32_16x16x32_bf16 v[112:115], v[160:163], v[192:195], v[112:115]\n"
    "ds_read_b128 v[88:91], v245 offset:2048\n"
    "v_mfma_f32_16x16x32_bf16 v[116:119], v[164:167], v[192:195], v[116:119]\n"
    "ds_read_b128 v[92:95], v245 offset:3072\n"
    "v_mfma_f32_16x16x32_bf16 v[120:123], v[168:171], v[192:195], v[120:123]\n"
    "ds_read_b128 v[96:99], v246 offset:0\n"
    "v_mfma_f32_16x16x32_bf16 v[124:127], v[172:175], v[192:195], v[124:127]\n"
    "ds_read_b128 v[100:103], v246 offset:1024\n"
    "ds_read_b128 v[104:107], v246 offset:2048\n"
    "ds_read_b128 v[108:111], v246 offset:3072\n"
    "s_waitcnt lgkmcnt(7)\n"
    "v_pk_add_f32 v[64:65], v[64:65], v[80:81]\n"
    "v_pk_add_f32 v[66:67], v[66:67], v[82:83]\n"
    "s_waitcnt lgkmcnt(6)\n"
    "v_pk_add_f32 v[68:69], v[68:69], v[84:85]\n"
    "v_pk_add_f32 v[70:71], v[70:71], v[86:87]\n"
    "s_waitcnt lgkmcnt(5)\n"
    "v_pk_add_f32 v[72:73], v[72:73], v[88:89]\n"
    "v_pk_add_f32 v[74:75], v[74:75], v[90:91]\n"
    "s_waitcnt lgkmcnt(4)\n"
    "v_pk_add_f32 v[76:77], v[76:77], v[92:93]\n"
    "v_pk_add_f32 v[78:79], v[78:79], v[94:95]\n"
    "s_waitcnt lgkmcnt(3)\n"
    "v_pk_add_f32 v[64:65], v[64:65], v[96:97]\n"
    "v_pk_add_f32 v[66:67], v[66:67], v[98:99]\n"
    "s_waitcnt lgkmcnt(2)\n"
    "v_pk_add_f32 v[68:69], v[68:69], v[100:101]\n"
    "v_pk_add_f32 v[70:71], v[70:71], v[102:103]\n"
    "s_waitcnt lgkmcnt(1)\n"
    "v_pk_add_f32 v[72:73], v[72:73], v[104:105]\n"
    "v_pk_add_f32 v[74:75], v[74:75], v[106:107]\n"
    "s_waitcnt lgkmcnt(0)\n"
    "v_pk_add_f32 v[76:77], v[76:77], v[108:109]\n"
    "v_pk_add_f32 v[78:79], v[78:79], v[110:111]\n"
    "v_pk_add_f32 v[112:113], v[112:113], v[64:65]\n"
    "v_pk_add_f32 v[114:115], v[114:115], v[66:67]\n"
    "v_cvt_pk_bf16_f32 v24, v112, v113\n"
    "v_cvt_pk_bf16_f32 v25, v114, v115\n"
    "v_pk_add_f32 v[116:117], v[116:117], v[68:69]\n"
    "v_pk_add_f32 v[118:119], v[118:119], v[70:71]\n"
    "v_cvt_pk_bf16_f32 v26, v116, v117\n"
    "v_cvt_pk_bf16_f32 v27, v118, v119\n"
    "s_nop 1\n"
    "v_permlane16_swap_b32 v24, v26\n"
    "v_permlane16_swap_b32 v25, v27\n"
    "s_nop 0\n"
    "v_mov_b32_dpp v32, v24 row_ror:8 row_mask:0xf bank_mask:0xf\n"
    "v_mov_b32_dpp v33, v25 row_ror:8 row_mask:0xf bank_mask:0xf\n"
    "v_mov_b32_dpp v34, v26 row_ror:8 row_mask:0xf bank_mask:0xf\n"
    "v_mov_b32_dpp v35, v27 row_ror:8 row_mask:0xf bank_mask:0xf\n"
    "v_pk_add_f32 v[120:121], v[120:121], v[72:73]\n"
    "v_pk_add_f32 v[122:123], v[122:123], v[74:75]\n"
    "v_cvt_pk_bf16_f32 v28, v120, v121\n"
    "v_cvt_pk_bf16_f32 v29, v122, v123\n"
    "v_pk_add_f32 v[124:125], v[124:125], v[76:77]\n"
    "v_pk_add_f32 v[126:127], v[126:127], v[78:79]\n"
    "v_cvt_pk_bf16_f32 v30, v124, v125\n"
    "v_cvt_pk_bf16_f32 v31, v126, v127\n"
    "s_nop 1\n"
    "v_permlane16_swap_b32 v28, v30\n"
    "v_permlane16_swap_b32 v29, v31\n"
    "s_nop 0\n"
    "v_mov_b32_dpp v24, v28 row_ror:8 row_mask:0xf bank_mask:0xc\n"
    "v_mov_b32_dpp v25, v29 row_ror:8 row_mask:0xf bank_mask:0xc\n"
    "v_mov_b32_dpp v26, v30 row_ror:8 row_mask:0xf bank_mask:0xc\n"
    "v_mov_b32_dpp v27, v31 row_ror:8 row_mask:0xf bank_mask:0xc\n"
    "buffer_store_dwordx4 v[24:27], v248, s[92:95], 0 offen sc1\n"
    "v_mov_b32_dpp v28, v32 quad_perm:[0,1,2,3] row_mask:0xf bank_mask:0x3\n"
    "v_mov_b32_dpp v29, v33 quad_perm:[0,1,2,3] row_mask:0xf bank_mask:0x3\n"
    "v_mov_b32_dpp v30, v34 quad_perm:[0,1,2,3] row_mask:0xf bank_mask:0x3\n"
    "v_mov_b32_dpp v31, v35 quad_perm:[0,1,2,3] row_mask:0xf bank_mask:0x3\n"
    "buffer_store_dwordx4 v[28:31], v248, s[92:95], s12 offen sc1\n"
    "L_fin:\n"
    "s_endpgm\n"
    ".fill 34, 4, 0xbf800000\n"
    "L_cidle:\n"
    "v_bfe_u32 v3, v1, 2, 2\n"
    "v_lshl_add_u32 v6, v5, 3, v3\n"
    "v_lshrrev_b32 v3, 1, v3\n"
    "v_and_b32 v2, 1, v5\n"
    "v_lshl_or_b32 v3, v2, 1, v3\n"
    "v_lshlrev_b32 v3, 5, v3\n"
    "v_lshl_or_b32 v3, v6, 7, v3\n"
    "v_and_b32 v2, 3, v1\n"
    "v_lshl_or_b32 v8, v2, 3, v3\n"
    "v_xor_b32 v9, 32, v8\n"
    "v_xor_b32 v10, 64, v8\n"
    "v_xor_b32 v11, 96, v8\n"
    "v_add_u32 v12, 65536, v8\n"
    "v_add_u32 v13, 65536, v9\n"
    "v_add_u32 v14, 65536, v10\n"
    "v_add_u32 v15, 65536, v11\n"
    "v_add_u32 v16, 131072, v8\n"
    "v_add_u32 v17, 131072, v9\n"
    "v_add_u32 v18, 131072, v10\n"
    "v_add_u32 v19, 131072, v11\n"
    "s_lshl_b32 s51, s36, 12\n"
    "v_lshlrev_b32 v6, 9, v5\n"
    "v_sub_u32 v6, s51, v6\n"
    "v_add_u32 v8, v6, v8\n"
    "v_add_u32 v9, v6, v9\n"
    "v_add_u32 v10, v6, v10\n"
    "v_add_u32 v11, v6, v11\n"
    "s_lshl_b32 s56, s66, 11\n"
    "s_add_u32 s66, s66, 1\n"
    "s_cmp_eq_u32 s66, 7\n"
    "s_cselect_b32 s66, 0, s66\n"
    "s_lshl_b32 s57, s66, 11\n"
    "s_add_u32 s66, s66, 1\n"
    "s_cmp_eq_u32 s66, 7\n"
    "s_cselect_b32 s66, 0, s66\n"
    "s_lshl_b32 s58, s66, 11\n"
    "s_add_u32 s66, s66, 1\n"
    "s_cmp_eq_u32 s66, 7\n"
    "s_cselect_b32 s66, 0, s66\n"
    "s_lshl_b32 s59, s66, 11\n"
    "s_add_u32 s66, s66, 1\n"
    "s_cmp_eq_u32 s66, 7\n"
    "s_cselect_b32 s66, 0, s66\n"
    "s_lshl_b32 s60, s66, 11\n"
    "s_add_u32 s66, s66, 1\n"
    "s_cmp_eq_u32 s66, 7\n"
    "s_cselect_b32 s66, 0, s66\n"
    "s_lshl_b32 s61, s66, 11\n"
    "s_add_u32 s66, s66, 1\n"
    "s_cmp_eq_u32 s66, 7\n"
    "s_cselect_b32 s66, 0, s66\n"
    "s_lshl_b32 s62, s66, 11\n"
    "s_waitcnt lgkmcnt(0)\n"
    "s_add_u32 s64, s6, 7409664\n"
    "s_addc_u32 s65, s7, 0\n"
    "global_load_dwordx2 v[252:253], v7, s[64:65] sc1\n"
    "s_add_u32 s67, s36, 1\n"
    "s_and_b32 s67, s67, 3\n"
    "s_mul_i32 s16, s67, 57344\n"
    "s_add_u32 s67, s36, 3\n"
    "s_and_b32 s67, s67, 3\n"
    "s_lshl_b32 s67, s67, 12\n"
    "s_add_u32 s8, s67, 65536\n"
    "s_add_u32 s67, s36, 2\n"
    "s_and_b32 s67, s67, 3\n"
    "s_mul_i32 s17, s67, 57344\n"
    "s_add_u32 s67, s36, 2\n"
    "s_and_b32 s67, s67, 3\n"
    "s_lshl_b32 s67, s67, 12\n"
    "s_add_u32 s9, s67, 81920\n"
    "s_add_u32 s67, s36, 3\n"
    "s_and_b32 s67, s67, 3\n"
    "s_mul_i32 s18, s67, 57344\n"
    "s_add_u32 s67, s36, 1\n"
    "s_and_b32 s67, s67, 3\n"
    "s_lshl_b32 s67, s67, 12\n"
    "s_add_u32 s10, s67, 98304\n"
    "s_add_u32 s67, s36, 4\n"
    "s_and_b32 s67, s67, 3\n"
    "s_mul_i32 s19, s67, 57344\n"
    "s_add_u32 s67, s36, 0\n"
    "s_and_b32 s67, s67, 3\n"
    "s_lshl_b32 s67, s67, 12\n"
    "s_add_u32 s11, s67, 65536\n"
    "s_mul_i32 s51, s43, 224\n"
    "s_mul_i32 s68, s36, 14\n"
    "s_add_u32 s51, s51, s68\n"
    "s_lshl_b32 s51, s51, 10\n"
    "s_add_u32 s20, s6, s51\n"
    "s_addc_u32 s21, s7, 0\n"
    "s_mov_b32 s22, 186368\n"
    "s_mov_b32 s23, 0x20000\n"
    "s_lshl_b32 s67, s43, 2\n"
    "s_add_u32 s67, s67, s36\n"
    "s_lshl_b32 s67, s67, 14\n"
    "s_lshl_b32 s68, s44, 7\n"
    "s_add_u32 s67, s67, s68\n"
    "s_add_u32 s92, s4, s67\n"
    "s_addc_u32 s93, s5, 0\n"
    "s_sub_u32 s94, 2097152, s67\n"
    "s_mov_b32 s95, 0x20000\n"
    "s_mov_b32 s12, 8192\n"
    "v_add_u32 v20, s16, v4\n"
    "v_add_u32 v21, s17, v4\n"
    "v_add_u32 v22, s18, v4\n"
    "v_add_u32 v23, s19, v4\n"
    "s_waitcnt vmcnt(0)\n"
    "s_barrier\n"
    "ds_read_b64_tr_b16 v[128:129], v8 offset:0\n"
    "ds_read_b64_tr_b16 v[130:131], v8 offset:2048\n"
    "ds_read_b64_tr_b16 v[132:133], v9 offset:0\n"
    "ds_read_b64_tr_b16 v[134:135], v9 offset:2048\n"
    "ds_read_b64_tr_b16 v[136:137], v10 offset:0\n"
    "ds_read_b64_tr_b16 v[138:139], v10 offset:2048\n"
    "ds_read_b64_tr_b16 v[140:141], v11 offset:0\n"
    "ds_read_b64_tr_b16 v[142:143], v11 offset:2048\n"
    "ds_read_b64_tr_b16 v[144:145], v8 offset:16384\n"
    "ds_read_b64_tr_b16 v[146:147], v8 offset:18432\n"
    "ds_read_b64_tr_b16 v[148:149], v9 offset:16384\n"
    "ds_read_b64_tr_b16 v[150:151], v9 offset:18432\n"
    "ds_read_b64_tr_b16 v[152:153], v10 offset:16384\n"
    "ds_read_b64_tr_b16 v[154:155], v10 offset:18432\n"
    "ds_read_b64_tr_b16 v[156:157], v11 offset:16384\n"
    "ds_read_b64_tr_b16 v[158:159], v11 offset:18432\n"
    "ds_read_b64_tr_b16 v[160:161], v8 offset:32768\n"
    "ds_read_b64_tr_b16 v[162:163], v8 offset:34816\n"
    "ds_read_b64_tr_b16 v[164:165], v9 offset:32768\n"
    "ds_read_b64_tr_b16 v[166:167], v9 offset:34816\n"
    "ds_read_b64_tr_b16 v[168:169], v10 offset:32768\n"
    "ds_read_b64_tr_b16 v[170:171], v10 offset:34816\n"
    "ds_read_b64_tr_b16 v[172:173], v11 offset:32768\n"
    "ds_read_b64_tr_b16 v[174:175], v11 offset:34816\n"
    "ds_read_b64_tr_b16 v[176:177], v8 offset:49152\n"
    "ds_read_b64_tr_b16 v[178:179], v8 offset:51200\n"
    "ds_read_b64_tr_b16 v[180:181], v9 offset:49152\n"
    "ds_read_b64_tr_b16 v[182:183], v9 offset:51200\n"
    "ds_read_b64_tr_b16 v[184:185], v10 offset:49152\n"
    "ds_read_b64_tr_b16 v[186:187], v10 offset:51200\n"
    "ds_read_b64_tr_b16 v[188:189], v11 offset:49152\n"
    "ds_read_b64_tr_b16 v[190:191], v11 offset:51200\n"
    "s_waitcnt lgkmcnt(0)\n"
    "s_barrier\n"
    "s_cmp_eq_u32 s36, 0\n"
    "s_cbranch_scc0 L_icpok\n"
    "v_readfirstlane_b32 s40, v252\n"
    "v_readfirstlane_b32 s41, v253\n"
    "s_add_u32 s40, s40, 1\n"
    "s_addc_u32 s41, s41, 0\n"
    "v_lshlrev_b32 v249, 3, v1\n"
    "v_mov_b32 v250, 0\n"
    "v_mov_b32 v251, 0\n"
    "s_lshr_b32 s67, s43, 1\n"
    "s_lshl_b32 s67, s67, 12\n"
    "s_and_b32 s68, s43, 1\n"
    "s_lshl_b32 s68, s68, 3\n"
    "s_add_u32 s68, s68, s44\n"
    "s_and_b32 s68, s68, 3\n"
    "s_lshl_b32 s68, s68, 8\n"
    "s_add_u32 s67, s67, s68\n"
    "s_add_u32 s67, s67, 7340032\n"
    "s_add_u32 s64, s6, s67\n"
    "s_addc_u32 s65, s7, 0\n"
    "s_sleep 35\n"
    "s_mov_b32 s63, 0x40000\n"
    "s_mov_b64 exec, 16383\n"
    "global_atomic_or_x2 v[252:253], v249, v[250:251], s[64:65] sc0 sc1\n"
    "L_icp:\n"
    "s_waitcnt vmcnt(0)\n"
    "v_cmp_ne_u64 vcc, s[40:41], v[252:253]\n"
    "s_nop 1\n"
    "s_cmp_eq_u64 vcc, 0\n"
    "s_cbranch_scc1 L_icpok\n"
    "s_sleep 1\n"
    "global_atomic_or_x2 v[252:253], v249, v[250:251], s[64:65] sc0 sc1\n"
    "s_sub_u32 s63, s63, 1\n"
    "s_cmp_eq_u32 s63, 0\n"
    "s_cbranch_scc0 L_icp\n"
    "s_waitcnt vmcnt(0)\n"
    "L_icpok:\n"
    "s_mov_b64 exec, -1\n"
    "s_branch L_p2b\n"
    "L_lidle2:\n"
    "v_and_b32 v5, 6, v2\n"
    "v_xor_b32 v5, v5, v3\n"
    "v_lshlrev_b32 v5, 4, v5\n"
    "v_lshl_or_b32 v16, v2, 10, v5\n"
    "v_add_u32 v17, 7168, v16\n"
    "v_add_u32 v18, 14336, v16\n"
    "v_add_u32 v19, 21504, v16\n"
    "s_lshl_b32 s51, s44, 7\n"
    "s_add_u32 s24, s14, s51\n"
    "s_addc_u32 s25, s15, 0\n"
    "s_sub_u32 s26, 1835008, s51\n"
    "s_mov_b32 s27, 0x20000\n"
    "s_mul_i32 s69, s49, 7\n"
    "s_add_u32 s68, s66, s69\n"
    "s_lshl_b32 s67, s68, 16\n"
    "s_and_b32 s68, s68, 1\n"
    "s_lshl_b32 s68, s68, 15\n"
    "s_sub_u32 s56, s67, s68\n"
    "s_add_u32 s66, s66, 1\n"
    "s_cmp_eq_u32 s66, 7\n"
    "s_cselect_b32 s66, 0, s66\n"
    "s_add_u32 s68, s66, s69\n"
    "s_lshl_b32 s67, s68, 16\n"
    "s_and_b32 s68, s68, 1\n"
    "s_lshl_b32 s68, s68, 15\n"
    "s_sub_u32 s57, s67, s68\n"
    "s_add_u32 s66, s66, 1\n"
    "s_cmp_eq_u32 s66, 7\n"
    "s_cselect_b32 s66, 0, s66\n"
    "s_add_u32 s68, s66, s69\n"
    "s_lshl_b32 s67, s68, 16\n"
    "s_and_b32 s68, s68, 1\n"
    "s_lshl_b32 s68, s68, 15\n"
    "s_sub_u32 s58, s67, s68\n"
    "s_add_u32 s66, s66, 1\n"
    "s_cmp_eq_u32 s66, 7\n"
    "s_cselect_b32 s66, 0, s66\n"
    "s_add_u32 s68, s66, s69\n"
    "s_lshl_b32 s67, s68, 16\n"
    "s_and_b32 s68, s68, 1\n"
    "s_lshl_b32 s68, s68, 15\n"
    "s_sub_u32 s59, s67, s68\n"
    "s_add_u32 s66, s66, 1\n"
    "s_cmp_eq_u32 s66, 7\n"
    "s_cselect_b32 s66, 0, s66\n"
    "s_add_u32 s68, s66, s69\n"
    "s_lshl_b32 s67, s68, 16\n"
    "s_and_b32 s68, s68, 1\n"
    "s_lshl_b32 s68, s68, 15\n"
    "s_sub_u32 s60, s67, s68\n"
    "s_add_u32 s66, s66, 1\n"
    "s_cmp_eq_u32 s66, 7\n"
    "s_cselect_b32 s66, 0, s66\n"
    "s_add_u32 s68, s66, s69\n"
    "s_lshl_b32 s67, s68, 16\n"
    "s_and_b32 s68, s68, 1\n"
    "s_lshl_b32 s68, s68, 15\n"
    "s_sub_u32 s61, s67, s68\n"
    "s_add_u32 s66, s66, 1\n"
    "s_cmp_eq_u32 s66, 7\n"
    "s_cselect_b32 s66, 0, s66\n"
    "s_add_u32 s68, s66, s69\n"
    "s_lshl_b32 s67, s68, 16\n"
    "s_and_b32 s68, s68, 1\n"
    "s_lshl_b32 s68, s68, 15\n"
    "s_sub_u32 s62, s67, s68\n"
    "s_mov_b32 s46, s56\n"
    "s_add_u32 m0, s48, 0\n"
    "s_nop 0\n"
    "buffer_load_dwordx4 v16, s[24:27], s46 offen offset:0 lds\n"
    "buffer_load_dwordx4 v17, s[24:27], s46 offen offset:1024 lds\n"
    "buffer_load_dwordx4 v18, s[24:27], s46 offen offset:2048 lds\n"
    "buffer_load_dwordx4 v19, s[24:27], s46 offen offset:3072 lds\n"
    "s_mov_b32 s46, s57\n"
    "s_add_u32 m0, s48, 16384\n"
    "s_nop 0\n"
    "buffer_load_dwordx4 v16, s[24:27], s46 offen offset:0 lds\n"
    "buffer_load_dwordx4 v17, s[24:27], s46 offen offset:1024 lds\n"
    "buffer_load_dwordx4 v18, s[24:27], s46 offen offset:2048 lds\n"
    "buffer_load_dwordx4 v19, s[24:27], s46 offen offset:3072 lds\n"
    "s_mov_b32 s46, s58\n"
    "s_add_u32 m0, s48, 32768\n"
    "s_nop 0\n"
    "buffer_load_dwordx4 v16, s[24:27], s46 offen offset:0 lds\n"
    "buffer_load_dwordx4 v17, s[24:27], s46 offen offset:1024 lds\n"
    "buffer_load_dwordx4 v18, s[24:27], s46 offen offset:2048 lds\n"
    "s_waitcnt vmcnt(10)\n"
    "buffer_load_dwordx4 v19, s[24:27], s46 offen offset:3072 lds\n"
    "s_mov_b32 s46, s59\n"
    "s_add_u32 m0, s48, 49152\n"
    "s_waitcnt vmcnt(10)\n"
    "buffer_load_dwordx4 v16, s[24:27], s46 offen offset:0 lds\n"
    "s_waitcnt vmcnt(10)\n"
    "buffer_load_dwordx4 v17, s[24:27], s46 offen offset:1024 lds\n"
    "s_waitcnt vmcnt(10)\n"
    "buffer_load_dwordx4 v18, s[24:27], s46 offen offset:2048 lds\n"
    "s_waitcnt vmcnt(10)\n"
    "buffer_load_dwordx4 v19, s[24:27], s46 offen offset:3072 lds\n"
    "s_mov_b32 s46, s60\n"
    "s_add_u32 m0, s48, 65536\n"
    "s_waitcnt vmcnt(10)\n"
    "buffer_load_dwordx4 v16, s[24:27], s46 offen offset:0 lds\n"
    "s_waitcnt vmcnt(10)\n"
    "buffer_load_dwordx4 v17, s[24:27], s46 offen offset:1024 lds\n"
    "s_waitcnt vmcnt(10)\n"
    "buffer_load_dwordx4 v18, s[24:27], s46 offen offset:2048 lds\n"
    "s_waitcnt vmcnt(10)\n"
    "buffer_load_dwordx4 v19, s[24:27], s46 offen offset:3072 lds\n"
    "s_mov_b32 s46, s61\n"
    "s_add_u32 m0, s48, 81920\n"
    "s_waitcnt vmcnt(10)\n"
    "buffer_load_dwordx4 v16, s[24:27], s46 offen offset:0 lds\n"
    "s_waitcnt vmcnt(10)\n"
    "buffer_load_dwordx4 v17, s[24:27], s46 offen offset:1024 lds\n"
    "s_waitcnt vmcnt(10)\n"
    "buffer_load_dwordx4 v18, s[24:27], s46 offen offset:2048 lds\n"
    "s_waitcnt vmcnt(10)\n"
    "buffer_load_dwordx4 v19, s[24:27], s46 offen offset:3072 lds\n"
    "s_mov_b32 s46, s62\n"
    "s_add_u32 m0, s48, 98304\n"
    "s_waitcnt vmcnt(10)\n"
    "buffer_load_dwordx4 v16, s[24:27], s46 offen offset:0 lds\n"
    "s_waitcnt vmcnt(10)\n"
    "buffer_load_dwordx4 v17, s[24:27], s46 offen offset:1024 lds\n"
    "s_waitcnt vmcnt(10)\n"
    "buffer_load_dwordx4 v18, s[24:27], s46 offen offset:2048 lds\n"
    "s_barrier\n"
    "s_waitcnt vmcnt(10)\n"
    "buffer_load_dwordx4 v19, s[24:27], s46 offen offset:3072 lds\n"
    "s_add_u32 s46, s56, 65536\n"
    "s_add_u32 m0, s48, 114688\n"
    "s_waitcnt vmcnt(10)\n"
    "buffer_load_dwordx4 v16, s[24:27], s46 offen offset:0 lds\n"
    "s_waitcnt vmcnt(10)\n"
    "buffer_load_dwordx4 v17, s[24:27], s46 offen offset:1024 lds\n"
    "s_waitcnt vmcnt(10)\n"
    "buffer_load_dwordx4 v18, s[24:27], s46 offen offset:2048 lds\n"
    "s_waitcnt vmcnt(10)\n"
    "buffer_load_dwordx4 v19, s[24:27], s46 offen offset:3072 lds\n"
    "s_add_u32 s46, s57, 65536\n"
    "s_add_u32 m0, s48, 131072\n"
    "s_waitcnt vmcnt(10)\n"
    "buffer_load_dwordx4 v16, s[24:27], s46 offen offset:0 lds\n"
    "s_waitcnt vmcnt(10)\n"
    "buffer_load_dwordx4 v17, s[24:27], s46 offen offset:1024 lds\n"
    "s_waitcnt vmcnt(10)\n"
    "buffer_load_dwordx4 v18, s[24:27], s46 offen offset:2048 lds\n"
    "s_waitcnt vmcnt(10)\n"
    "buffer_load_dwordx4 v19, s[24:27], s46 offen offset:3072 lds\n"
    "s_barrier\n"
    "s_add_u32 s46, s59, 65536\n"
    "s_add_u32 m0, s48, 0\n"
    "s_waitcnt vmcnt(10)\n"
    "buffer_load_dwordx4 v16, s[24:27], s46 offen offset:0 lds\n"
    "s_waitcnt vmcnt(10)\n"
    "buffer_load_dwordx4 v17, s[24:27], s46 offen offset:1024 lds\n"
    "s_waitcnt vmcnt(10)\n"
    "buffer_load_dwordx4 v18, s[24:27], s46 offen offset:2048 lds\n"
    "s_waitcnt vmcnt(10)\n"
    "buffer_load_dwordx4 v19, s[24:27], s46 offen offset:3072 lds\n"
    "s_add_u32 s46, s60, 65536\n"
    "s_add_u32 m0, s48, 16384\n"
    "s_waitcnt vmcnt(10)\n"
    "buffer_load_dwordx4 v16, s[24:27], s46 offen offset:0 lds\n"
    "s_waitcnt vmcnt(10)\n"
    "buffer_load_dwordx4 v17, s[24:27], s46 offen offset:1024 lds\n"
    "s_waitcnt vmcnt(10)\n"
    "buffer_load_dwordx4 v18, s[24:27], s46 offen offset:2048 lds\n"
    "s_waitcnt vmcnt(10)\n"
    "buffer_load_dwordx4 v19, s[24:27], s46 offen offset:3072 lds\n"
    "s_add_u32 s46, s58, 65536\n"
    "s_add_u32 m0, s48, 147456\n"
    "s_waitcnt vmcnt(10)\n"
    "buffer_load_dwordx4 v16, s[24:27], s46 offen offset:0 lds\n"
    "s_waitcnt vmcnt(10)\n"
    "buffer_load_dwordx4 v17, s[24:27], s46 offen offset:1024 lds\n"
    "s_waitcnt vmcnt(10)\n"
    "buffer_load_dwordx4 v18, s[24:27], s46 offen offset:2048 lds\n"
    "s_waitcnt vmcnt(10)\n"
    "buffer_load_dwordx4 v19, s[24:27], s46 offen offset:3072 lds\n"
    "s_add_u32 s46, s61, 65536\n"
    "s_add_u32 m0, s48, 32768\n"
    "s_waitcnt vmcnt(10)\n"
    "buffer_load_dwordx4 v16, s[24:27], s46 offen offset:0 lds\n"
    "s_waitcnt vmcnt(10)\n"
    "buffer_load_dwordx4 v17, s[24:27], s46 offen offset:1024 lds\n"
    "s_waitcnt vmcnt(10)\n"
    "buffer_load_dwordx4 v18, s[24:27], s46 offen offset:2048 lds\n"
    "s_waitcnt vmcnt(10)\n"
    "buffer_load_dwordx4 v19, s[24:27], s46 offen offset:3072 lds\n"
    "s_add_u32 s46, s62, 65536\n"
    "s_add_u32 m0, s48, 49152\n"
    "s_waitcnt vmcnt(10)\n"
    "buffer_load_dwordx4 v16, s[24:27], s46 offen offset:0 lds\n"
    "s_waitcnt vmcnt(10)\n"
    "buffer_load_dwordx4 v17, s[24:27], s46 offen offset:1024 lds\n"
    "s_waitcnt vmcnt(10)\n"
    "buffer_load_dwordx4 v18, s[24:27], s46 offen offset:2048 lds\n"
    "s_waitcnt vmcnt(10)\n"
    "buffer_load_dwordx4 v19, s[24:27], s46 offen offset:3072 lds\n"
    "v_readfirstlane_b32 s40, v20\n"
    "v_readfirstlane_b32 s41, v21\n"
    "s_add_u32 s40, s40, 1\n"
    "s_addc_u32 s41, s41, 0\n"
    "v_lshlrev_b32 v249, 3, v1\n"
    "v_mov_b32 v250, 0\n"
    "v_mov_b32 v251, 0\n"
    "s_cmp_eq_u32 s49, 0\n"
    "s_cbranch_scc0 L_ipaok\n"
    "s_lshl_b32 s51, s37, 2\n"
    "s_add_u32 s51, s51, s38\n"
    "s_lshl_b32 s51, s51, 3\n"
    "s_add_u32 s51, s51, 7405344\n"
    "s_add_u32 s68, s6, s51\n"
    "s_addc_u32 s69, s7, 0\n"
    "v_mov_b32 v2, s40\n"
    "v_mov_b32 v3, s41\n"
    "s_mov_b64 exec, 1\n"
    "global_atomic_swap_x2 v7, v[2:3], s[68:69] sc1\n"
    "s_mov_b64 exec, -1\n"
    "L_ipaok:\n"
    "s_barrier\n"
    "s_barrier\n"
    "s_waitcnt vmcnt(0)\n"
    "s_branch L_hlp\n"
    "L_own:\n"
    "s_add_u32 s68, s6, 7405568\n"
    "s_addc_u32 s69, s7, 0\n"
    "s_mov_b32 s63, 0x40000\n"
    "v_mov_b32 v2, s40\n"
    "v_mov_b32 v3, s41\n"
    "s_mov_b32 exec_hi, 0xffff\n"
    "L_upd:\n"
    "global_atomic_or_x2 v[8:9], v249, v[250:251], s[68:69] offset:0 sc0 sc1\n"
    "s_waitcnt vmcnt(0)\n"
    "v_cmp_ne_u64 vcc, s[40:41], v[8:9]\n"
    "s_nop 1\n"
    "s_cmp_eq_u64 vcc, 0\n"
    "s_cbranch_scc1 L_updok\n"
    "s_sleep 8\n"
    "s_sub_u32 s63, s63, 1\n"
    "s_cmp_eq_u32 s63, 0\n"
    "s_cbranch_scc0 L_upd\n"
    "L_updok:\n"
    "s_add_u32 s64, s6, 7409664\n"
    "s_addc_u32 s65, s7, 0\n"
    "s_mov_b64 exec, 1\n"
    "global_atomic_swap_x2 v7, v[2:3], s[64:65] sc1\n"
    "s_mov_b64 exec, -1\n"
    "s_branch L_fin\n"
    "L_kend:\n"
    : "+v"(tid)
    : "s"(__builtin_amdgcn_kernarg_segment_ptr()), "s"(blockIdx.x), "s"((unsigned)(size_t)lds)
    : "memory", "vcc", "scc", "v1", "v2", "v3", "v4", "v5", "v6", "v7", "v8", "v9", "v10", "v11", "v12", "v13", "v14", "v15", "v16", "v17", "v18", "v19", "v20", "v21", "v22", "v23", "v24", "v25", "v26", "v27", "v28", "v29", "v30", "v31", "v32", "v33", "v34", "v35", "v36", "v37", "v38", "v39", "v40", "v41", "v42", "v43", "v44", "v45", "v46", "v47", "v48", "v49", "v50", "v51", "v52", "v53", "v54", "v55", "v56", "v57", "v58", "v59", "v60", "v61", "v62", "v63", "v64", "v65", "v66", "v67", "v68", "v69", "v70", "v71", "v72", "v73", "v74", "v75", "v76", "v77", "v78", "v79", "v80", "v81", "v82", "v83", "v84", "v85", "v86", "v87", "v88", "v89", "v90", "v91", "v92", "v93", "v94", "v95", "v96", "v97", "v98", "v99", "v100", "v101", "v102", "v103", "v104", "v105", "v106", "v107", "v108", "v109", "v110", "v111", "v112", "v113", "v114", "v115", "v116", "v117", "v118", "v119", "v120", "v121", "v122", "v123", "v124", "v125", "v126", "v127", "v128", "v129", "v130", "v131", "v132", "v133", "v134", "v135", "v136", "v137", "v138", "v139", "v140", "v141", "v142", "v143", "v144", "v145", "v146", "v147", "v148", "v149", "v150", "v151", "v152", "v153", "v154", "v155", "v156", "v157", "v158", "v159", "v160", "v161", "v162", "v163", "v164", "v165", "v166", "v167", "v168", "v169", "v170", "v171", "v172", "v173", "v174", "v175", "v176", "v177", "v178", "v179", "v180", "v181", "v182", "v183", "v184", "v185", "v186", "v187", "v188", "v189", "v190", "v191", "v192", "v193", "v194", "v195", "v196", "v197", "v198", "v199", "v200", "v201", "v202", "v203", "v204", "v205", "v206", "v207", "v208", "v209", "v210", "v211", "v212", "v213", "v214", "v215", "v216", "v217", "v218", "v219", "v220", "v221", "v222", "v223", "v224", "v225", "v226", "v227", "v228", "v229", "v230", "v231", "v232", "v233", "v234", "v235", "v236", "v237", "v238", "v239", "v240", "v241", "v242", "v243", "v244", "v245", "v246", "v247", "v248", "v249", "v250", "v251", "v252", "v253", "v254", "v255", "s4", "s5", "s6", "s7", "s8", "s9", "s10", "s11", "s12", "s13", "s14", "s15", "s16", "s17", "s18", "s19", "s20", "s21", "s22", "s23", "s24", "s25", "s26", "s27", "s28", "s29", "s30", "s31", "s36", "s37", "s38", "s39", "s40", "s41", "s42", "s43", "s44", "s45", "s46", "s47", "s48", "s49", "s50", "s51", "s52", "s53", "s54", "s55", "s56", "s57", "s58", "s59", "s60", "s61", "s62", "s63", "s64", "s65", "s66", "s67", "s68", "s69", "s70", "s71", "s72", "s73", "s74", "s75", "s76", "s77", "s78", "s79", "s80", "s81", "s82", "s83", "s84", "s85", "s86", "s87", "s88", "s89", "s90", "s91", "s92", "s93", "s94", "s95", "s96", "s97", "s98", "s99");
}

extern "C" void kernel_launch(void* const* d_in, const int* in_sizes, int n_in, void* d_out, int out_size, void* d_ws, size_t ws_size,
                              hipStream_t stream) {
  mish_mlp_kernel<<<256, 512, 0, stream>>>(d_in[0], d_in[1], d_in[2], d_in[3], d_out, d_ws);
}
```
